```python
import torch

def _softmax(x, axis=-1):
    return torch.softmax(x, dim=axis)
def _gelu(x):
    return torch.nn.functional.gelu(x, approximate="tanh")
def _silu(x):
    return torch.nn.functional.silu(x)
def _sigmoid(x):
    return torch.sigmoid(x)
def _layernorm(x, axis=-1, eps=1e-5):
    m = x.mean(dim=axis, keepdim=True)
    v = x.var(dim=axis, keepdim=True, unbiased=False)
    return (x - m) / torch.sqrt(v + eps)
def _scatter_add(tgt, idx, src, axis=0):
    return tgt.clone().index_add_(axis, idx, src)
def _segment_sum(src, seg, n):
    out = torch.zeros((n,) + src.shape[1:], dtype=src.dtype)
    return out.index_add_(0, seg, src)
def _pad(x, pw):
    flat = tuple(v for pair in reversed(pw) for v in pair)
    return torch.nn.functional.pad(x, flat)

kernel_name = 'recurrent_scan__gqa_block_cached__mixer_token_mlp__decode'

def setup_inputs():

    _w = (lambda a: (lambda t: t.to(torch.bfloat16) if t.is_floating_point() else t)(torch.as_tensor(a)))
    def _A_setup():
        B, Sq, D = 1, 1, 512
        Hq, Hkv, Sk, Dh = 32, 8, 65536, 128
        assert Hq % Hkv == 0, (Hq, Hkv)
        return {
            "x": _w((lambda *s: torch.randn(*s, dtype=torch.bfloat16) * 0.4)(B, Sq, D)),
            "k_cache": _w((lambda *s: torch.randn(*s, dtype=torch.bfloat16) * 3.2)(B, Hkv, Sk, Dh)),
            "v_cache": _w((lambda *s: torch.randn(*s, dtype=torch.bfloat16) * 1.6)(B, Hkv, Sk, Dh)),
            "wq": _w((lambda *s: torch.randn(*s, dtype=torch.bfloat16) * 3.2)(D, Hq * Dh) * (D ** -0.5)),
            "wo": _w((lambda *s: torch.randn(*s, dtype=torch.bfloat16) * 1.6)(Hq * Dh, D) * ((Hq * Dh) ** -0.5)),
        }
    _A_in = _A_setup()

    def _B_setup():
        B, S, D, F = 1, 1, 512, 1792
        return {
            "x": _w((lambda *s: torch.randn(*s, dtype=torch.bfloat16) * 0.4)(B, S, D)),
            "w1": _w((lambda *s: torch.randn(*s, dtype=torch.bfloat16) * 1.6)(S, F) * (S ** -0.5)),
            "b1": _w((lambda *s: torch.randn(*s, dtype=torch.bfloat16) * 1.6)(F) * 0.02),
            "w2": _w((lambda *s: torch.randn(*s, dtype=torch.bfloat16) * 1.6)(F, S) * (F ** -0.5)),
            "b2": _w((lambda *s: torch.randn(*s, dtype=torch.bfloat16) * 1.6)(S) * 0.02),
        }
    _B_in = _B_setup()

    return {
        "A_x": _A_in["x"],
        "A_k_cache": _A_in["k_cache"],
        "A_v_cache": _A_in["v_cache"],
        "A_wq": _A_in["wq"],
        "A_wo": _A_in["wo"],
        "B_x": _B_in["x"],
        "B_w1": _B_in["w1"],
        "B_b1": _B_in["b1"],
        "B_w2": _B_in["w2"],
        "B_b2": _B_in["b2"],
    }

def reference(A_x, A_k_cache, A_v_cache, A_wq, A_wo, B_x, B_w1, B_b1, B_w2, B_b2):
    def _A_ref(x, k_cache, v_cache, wq, wo):
        B, Sq, D = x.shape
        Hq, Dh = 32, 128
        Hkv = k_cache.shape[1]
        rep = Hq // Hkv
        q = (lambda x, *d: x.permute(*d))((x @ wq).reshape(B, Sq, Hq, Dh), 0, 2, 1, 3)
        k = (lambda x, n, axis: torch.repeat_interleave(x, n, dim=axis))(k_cache, rep, axis=1)
        v = (lambda x, n, axis: torch.repeat_interleave(x, n, dim=axis))(v_cache, rep, axis=1)
        scores = torch.einsum("bhqd,bhkd->bhqk", q, k) * (Dh ** -0.5)
        p = _softmax(scores, axis=-1)
        o = torch.einsum("bhqk,bhkd->bhqd", p, v)
        return (lambda x, *d: x.permute(*d))(o, 0, 2, 1, 3).reshape(B, Sq, Hq * Dh) @ wo

    def _B_ref(x, w1, b1, w2, b2):
        B, S, D = x.shape
        h = _layernorm(x, axis=-1)
        u = torch.einsum("bsd,sf->bdf", h, w1) + b1
        u = _gelu(u)
        y = torch.einsum("bdf,fs->bds", u, w2) + b2
        return (lambda x, *d: x.permute(*d))(y, 0, 2, 1)

    x = A_x
    xn = _layernorm(x, axis=-1)
    s = _A_ref(xn, A_k_cache, A_v_cache, A_wq, A_wo)
    g = _silu(_B_ref(xn, B_w1, B_b1, B_w2, B_b2))
    return x + s * g
```

```cpp
#include <hip/hip_runtime.h>
#include <hip/hip_bf16.h>
#include <stdint.h>

#define MIMIC 1

typedef unsigned short u16;
using u32x4 = __attribute__((ext_vector_type(4))) unsigned;
using f32x4 = __attribute__((ext_vector_type(4))) float;
using bf16x2 = __attribute__((ext_vector_type(2))) __bf16;

constexpr int D = 512, HQ = 32, HKV = 8, SK = 65536, DH = 128, F = 1792;
constexpr int NSPLIT = 64;
constexpr int KEYS_PER_SPLIT = SK / NSPLIT;

__device__ __forceinline__ float bf2f(u16 b) { return __uint_as_float(((unsigned)b) << 16); }
__device__ __forceinline__ float rbf(float f) { return __bfloat162float(__float2bfloat16(f)); }
__device__ __forceinline__ float mr(float f) { return MIMIC ? rbf(f) : f; }

__device__ __forceinline__ float wave_sum(float v) {
#pragma unroll
  for (int m = 32; m >= 1; m >>= 1) v += __shfl_xor(v, m);
  return v;
}
__device__ __forceinline__ float wave_max(float v) {
#pragma unroll
  for (int m = 32; m >= 1; m >>= 1) v = fmaxf(v, __shfl_xor(v, m));
  return v;
}

__device__ __forceinline__ void ln512(const float* in, float* out, int lane) {
  float v[8]; float s = 0.f;
#pragma unroll
  for (int i = 0; i < 8; ++i) { v[i] = in[lane * 8 + i]; s += v[i]; }
  s = wave_sum(s);
  float mean = s * (1.f / 512.f);
  float q = 0.f;
#pragma unroll
  for (int i = 0; i < 8; ++i) { float d = v[i] - mean; q += d * d; }
  q = wave_sum(q);
  float var = mr(q * (1.f / 512.f));
  float m = mr(mean);
  float den = mr(sqrtf(mr(var + 1e-5f)));
#pragma unroll
  for (int i = 0; i < 8; ++i) out[lane * 8 + i] = mr(mr(v[i] - m) / den);
}

__global__ void __launch_bounds__(256) k_pre(const u16* __restrict__ x, const u16* __restrict__ wq, const u16* __restrict__ w1,
                                             const u16* __restrict__ b1, const u16* __restrict__ w2, const u16* __restrict__ b2,
                                             float* __restrict__ qout, float* __restrict__ gout) {
  __shared__ float xs[512], xn[512], hh[512];
  __shared__ float red[256 * 8];
  int tid = threadIdx.x, lane = tid & 63, wave = tid >> 6;
  for (int i = tid; i < 512; i += 256) xs[i] = bf2f(x[i]);
  __syncthreads();
  if (wave == 0) { ln512(xs, xn, lane); ln512(xn, hh, lane); }
  __syncthreads();
  if (blockIdx.x < 128) {
    int c0 = blockIdx.x * 32;
    int cg = tid & 3, kr = tid >> 2;
    float acc[8] = {0, 0, 0, 0, 0, 0, 0, 0};
    for (int k = kr; k < 512; k += 64) {
      u32x4 w = *reinterpret_cast<const u32x4*>(wq + (size_t)k * 4096 + c0 + cg * 8);
      float xv = xn[k];
#pragma unroll
      for (int j = 0; j < 4; ++j) {
        acc[2 * j] += xv * __uint_as_float(w[j] << 16);
        acc[2 * j + 1] += xv * __uint_as_float(w[j] & 0xffff0000u);
      }
    }
#pragma unroll
    for (int j = 0; j < 8; ++j) red[tid * 8 + j] = acc[j];
    __syncthreads();
    if (tid < 32) {
      int cgg = tid >> 3, j = tid & 7;
      float s = 0.f;
      for (int r = 0; r < 64; ++r) s += red[(r * 4 + cgg) * 8 + j];
      qout[c0 + tid] = rbf(s);
    }
  } else {
    int d = (blockIdx.x - 128) * 4 + wave;
    float h = hh[d];
    float s = 0.f;
    for (int f = lane; f < F; f += 64) {
      float u = mr(mr(h * bf2f(w1[f])) + bf2f(b1[f]));
      float inner = 0.7978845608028654f * (u + 0.044715f * u * u * u);
      float g = mr(0.5f * u * (1.f + tanhf(inner)));
      s += g * bf2f(w2[f]);
    }
    s = wave_sum(s);
    float y = mr(mr(s) + bf2f(b2[0]));
    float g = mr(y / (1.f + __expf(-y)));
    if (lane == 0) gout[d] = g;
  }
}

__device__ __forceinline__ float dot2(unsigned k, unsigned q, float acc) {
  return __builtin_amdgcn_fdot2_f32_bf16(__builtin_bit_cast(bf16x2, k), __builtin_bit_cast(bf16x2, q), acc, false);
}

__global__ void __launch_bounds__(256) k_attn(const u16* __restrict__ kc, const u16* __restrict__ vc, const float* __restrict__ q,
                                              float* __restrict__ pm, float* __restrict__ pl, float* __restrict__ po) {
  __shared__ float sm[4][4], sl[4][4], so[4][4][128];
  int s = blockIdx.x, kvh = blockIdx.y;
  int tid = threadIdx.x, lane = tid & 63, wave = tid >> 6, r = lane >> 4, c = lane & 15;
  unsigned qpk[4][4];
#pragma unroll
  for (int g = 0; g < 4; ++g)
#pragma unroll
    for (int k = 0; k < 4; ++k) {
      float lo = q[(kvh * 4 + g) * 128 + 8 * c + 2 * k], hi = q[(kvh * 4 + g) * 128 + 8 * c + 2 * k + 1];
      qpk[g][k] = (__float_as_uint(lo) >> 16) | (__float_as_uint(hi) & 0xffff0000u);
    }
  float m[4], l[4], acc[4][8];
#pragma unroll
  for (int g = 0; g < 4; ++g) { m[g] = -INFINITY; l[g] = 0.f;
#pragma unroll
    for (int k = 0; k < 8; ++k) acc[g][k] = 0.f; }
  const u16* kb = kc + ((size_t)kvh * SK + (size_t)s * KEYS_PER_SPLIT) * DH;
  const u16* vb = vc + ((size_t)kvh * SK + (size_t)s * KEYS_PER_SPLIT) * DH;
  for (int t0 = wave * 16; t0 < KEYS_PER_SPLIT; t0 += 64) {
    u32x4 K[4], V[4];
#pragma unroll
    for (int i = 0; i < 4; ++i) {
      K[i] = __builtin_nontemporal_load(reinterpret_cast<const u32x4*>(kb + (size_t)(t0 + 4 * i + r) * DH + 8 * c));
      V[i] = __builtin_nontemporal_load(reinterpret_cast<const u32x4*>(vb + (size_t)(t0 + 4 * i + r) * DH + 8 * c));
    }
#pragma unroll
    for (int i = 0; i < 4; ++i) {
#pragma unroll
      for (int g = 0; g < 4; ++g) {
        float d = 0.f;
#pragma unroll
        for (int k = 0; k < 4; ++k) d = dot2(K[i][k], qpk[g][k], d);
        d += __shfl_xor(d, 1); d += __shfl_xor(d, 2); d += __shfl_xor(d, 4); d += __shfl_xor(d, 8);
        float sc = mr(mr(d) * 0.08838834764831845f);
        float mn = fmaxf(m[g], sc);
        float alpha = __expf(m[g] - mn), p = __expf(sc - mn);
        l[g] = l[g] * alpha + p; m[g] = mn;
#pragma unroll
        for (int k = 0; k < 4; ++k) {
          acc[g][2 * k] = acc[g][2 * k] * alpha + p * __uint_as_float(V[i][k] << 16);
          acc[g][2 * k + 1] = acc[g][2 * k + 1] * alpha + p * __uint_as_float(V[i][k] & 0xffff0000u);
        }
      }
    }
  }
#pragma unroll
  for (int g = 0; g < 4; ++g) {
    float M = fmaxf(m[g], __shfl_xor(m[g], 16)); M = fmaxf(M, __shfl_xor(M, 32));
    float w = __expf(m[g] - M);
    float L = l[g] * w; L += __shfl_xor(L, 16); L += __shfl_xor(L, 32);
#pragma unroll
    for (int k = 0; k < 8; ++k) { float a = acc[g][k] * w; a += __shfl_xor(a, 16); a += __shfl_xor(a, 32); acc[g][k] = a; }
    if (r == 0) {
#pragma unroll
      for (int k = 0; k < 8; ++k) so[wave][g][8 * c + k] = acc[g][k];
      if (c == 0) { sm[wave][g] = M; sl[wave][g] = L; }
    }
  }
  __syncthreads();
  {
    int g = wave;
    float M = fmaxf(fmaxf(sm[0][g], sm[1][g]), fmaxf(sm[2][g], sm[3][g]));
    float L = 0.f, o0 = 0.f, o1 = 0.f;
#pragma unroll
    for (int w = 0; w < 4; ++w) { float wt = __expf(sm[w][g] - M); L += wt * sl[w][g]; o0 += wt * so[w][g][2 * lane]; o1 += wt * so[w][g][2 * lane + 1]; }
    int h = kvh * 4 + g;
    po[((size_t)h * NSPLIT + s) * 128 + 2 * lane] = o0;
    po[((size_t)h * NSPLIT + s) * 128 + 2 * lane + 1] = o1;
    if (lane == 0) { pm[h * NSPLIT + s] = M; pl[h * NSPLIT + s] = L; }
  }
}

__global__ void __launch_bounds__(256) k_comb(const float* __restrict__ pm, const float* __restrict__ pl, const float* __restrict__ po,
                                              const u16* __restrict__ wo, float* __restrict__ part) {
  __shared__ float o[128];
  __shared__ float red[32][64];
  int cb = blockIdx.x, h = blockIdx.y, tid = threadIdx.x;
  float M = -INFINITY;
  for (int s = 0; s < NSPLIT; ++s) M = fmaxf(M, pm[h * NSPLIT + s]);
  float L = 0.f;
  for (int s = 0; s < NSPLIT; ++s) L += __expf(pm[h * NSPLIT + s] - M) * pl[h * NSPLIT + s];
  if (tid < 128) {
    float a = 0.f;
    for (int s = 0; s < NSPLIT; ++s) a += __expf(pm[h * NSPLIT + s] - M) * po[((size_t)h * NSPLIT + s) * 128 + tid];
    o[tid] = mr(a / L);
  }
  __syncthreads();
  int cg = tid & 7, kr = tid >> 3;
  float acc[8] = {0, 0, 0, 0, 0, 0, 0, 0};
  for (int k = kr; k < 128; k += 32) {
    u32x4 w = *reinterpret_cast<const u32x4*>(wo + (size_t)(h * 128 + k) * 512 + cb * 64 + cg * 8);
    float ov = o[k];
#pragma unroll
    for (int j = 0; j < 4; ++j) {
      acc[2 * j] += ov * __uint_as_float(w[j] << 16);
      acc[2 * j + 1] += ov * __uint_as_float(w[j] & 0xffff0000u);
    }
  }
#pragma unroll
  for (int j = 0; j < 8; ++j) red[kr][cg * 8 + j] = acc[j];
  __syncthreads();
  if (tid < 64) {
    float s = 0.f;
    for (int r = 0; r < 32; ++r) s += red[r][tid];
    part[h * 512 + cb * 64 + tid] = s;
  }
}

__global__ void __launch_bounds__(512) k_final(const float* __restrict__ part, const float* __restrict__ g, const u16* __restrict__ x,
                                               __hip_bfloat16* __restrict__ out) {
  int d = threadIdx.x;
  float s = 0.f;
  for (int h = 0; h < 32; ++h) s += part[h * 512 + d];
  s = mr(s);
  out[d] = __float2bfloat16(bf2f(x[d]) + mr(s * g[d]));
}

extern "C" void kernel_launch(void* const* d_in, const int* in_sizes, int n_in, void* d_out, int out_size, void* d_ws, size_t ws_size,
                              hipStream_t stream) {
  const u16* x = (const u16*)d_in[0];
  const u16* kc = (const u16*)d_in[1];
  const u16* vc = (const u16*)d_in[2];
  const u16* wq = (const u16*)d_in[3];
  const u16* wo = (const u16*)d_in[4];
  const u16* w1 = (const u16*)d_in[6];
  const u16* b1 = (const u16*)d_in[7];
  const u16* w2 = (const u16*)d_in[8];
  const u16* b2 = (const u16*)d_in[9];
  float* ws = (float*)d_ws;
  float* q = ws;
  float* g = ws + 4096;
  float* pm = ws + 8192;
  float* pl = pm + 32 * NSPLIT;
  float* part = pl + 32 * NSPLIT;
  float* po = part + 32 * 512;
  k_pre<<<256, 256, 0, stream>>>(x, wq, w1, b1, w2, b2, q, g);
  k_attn<<<dim3(NSPLIT, HKV), 256, 0, stream>>>(kc, vc, q, pm, pl, po);
  k_comb<<<dim3(8, 32), 256, 0, stream>>>(pm, pl, po, wo, part);
  k_final<<<1, 512, 0, stream>>>(part, g, x, (__hip_bfloat16*)d_out);
}
```

```cpp
#include <hip/hip_runtime.h>
#include <hip/hip_bf16.h>
#include <stdint.h>

#define STAMPS 0
#define NSLOT 9
#define INFL 3

#define WQ_NT 1
#define ABL 0
#define POLL_SLEEP 2
#define QPOLL_SLEEP 8
#define LAS __attribute__((address_space(3)))
#define GAS __attribute__((address_space(1)))
typedef unsigned short u16;
typedef unsigned long long u64;
using u32x4 = __attribute__((ext_vector_type(4))) unsigned;
using f32x4 = __attribute__((ext_vector_type(4))) float;
using f32x2 = __attribute__((ext_vector_type(2))) float;
using bf16x2 = __attribute__((ext_vector_type(2))) __bf16;
using bf16x8 = __attribute__((ext_vector_type(8))) __bf16;
using s16x4 = __attribute__((ext_vector_type(4))) short;
using u32x2 = __attribute__((ext_vector_type(2))) unsigned;

constexpr int NC = 4;
constexpr int NWAVES = NC + 2;
constexpr int THREADS = NWAVES * 64;
constexpr int SLOT_BYTES = 16384;
constexpr int NQ = 64;
constexpr int RING = NSLOT * SLOT_BYTES;
constexpr int L_FULL = RING;
constexpr int L_FREE = RING + 64;
constexpr int L_MISC = RING + 128;
constexpr int L_QP = RING + 192;
constexpr int L_ML = RING + 448;
constexpr int L_RED = RING + 576;
constexpr int L_Q = RING + 1088;
constexpr int L_O = RING + 2112;
constexpr int LDS_BYTES = L_O + 8192;
static_assert(LDS_BYTES <= 163840, "LDS");
constexpr size_t W_EPOCH = 0;
constexpr size_t W_G0 = 4096;
constexpr size_t W_G1ML = W_G0 + 16384;
constexpr size_t W_G2 = W_G1ML + 16384;
constexpr size_t W_G1O = W_G2 + 524288;
constexpr size_t W_DBG = (size_t)256 << 20;

constexpr int SPIN_LDS = 1 << 20;
constexpr int SPIN_GLB = 1 << 15;
constexpr float LOG2E = 1.4426950408889634f;
constexpr float SCALE = 0.08838834764831845f;

__device__ __forceinline__ float bf2f(unsigned b) { return __uint_as_float(b << 16); }
__device__ __forceinline__ unsigned pk2(float lo, float hi) {
  f32x2 p = {lo, hi};
  return __builtin_bit_cast(unsigned, __builtin_convertvector(p, bf16x2));
}
__device__ __forceinline__ float rbf(float f) { return __uint_as_float(pk2(f, f) << 16); }
__device__ __forceinline__ float lo16(unsigned u) { return __uint_as_float(u << 16); }
__device__ __forceinline__ float hi16(unsigned u) { return __uint_as_float(u & 0xffff0000u); }

template <int CTRL> __device__ __forceinline__ float dpp(float x) {
  return __builtin_bit_cast(float, __builtin_amdgcn_mov_dpp(__builtin_bit_cast(int, x), CTRL, 0xf, 0xf, true));
}
constexpr int XOR1 = 0xB1, XOR2 = 0x4E, XOR7 = 0x141, XOR8 = 0x128;
__device__ __forceinline__ float x16(float x) {
  auto s = __builtin_amdgcn_permlane16_swap(__float_as_uint(x), __float_as_uint(x), false, false);
  return __uint_as_float((threadIdx.x & 16) ? s[0] : s[1]);
}
__device__ __forceinline__ float x32(float x) {
  auto s = __builtin_amdgcn_permlane32_swap(__float_as_uint(x), __float_as_uint(x), false, false);
  return __uint_as_float((threadIdx.x & 32) ? s[0] : s[1]);
}
__device__ __forceinline__ float sum_x16_x32(float x) {
  auto s = __builtin_amdgcn_permlane16_swap(__float_as_uint(x), __float_as_uint(x), false, false);
  x = __uint_as_float(s[0]) + __uint_as_float(s[1]);
  auto t = __builtin_amdgcn_permlane32_swap(__float_as_uint(x), __float_as_uint(x), false, false);
  return __uint_as_float(t[0]) + __uint_as_float(t[1]);
}
__device__ __forceinline__ float max_x16_x32(float x) {
  auto s = __builtin_amdgcn_permlane16_swap(__float_as_uint(x), __float_as_uint(x), false, false);
  x = fmaxf(__uint_as_float(s[0]), __uint_as_float(s[1]));
  auto t = __builtin_amdgcn_permlane32_swap(__float_as_uint(x), __float_as_uint(x), false, false);
  return fmaxf(__uint_as_float(t[0]), __uint_as_float(t[1]));
}
__device__ __forceinline__ float row_sum16(float x) {
  x += dpp<XOR1>(x); x += dpp<XOR2>(x); x += dpp<XOR7>(x); x += dpp<XOR8>(x);
  return x;
}
__device__ __forceinline__ float row_max16(float x) {
  x = fmaxf(x, dpp<XOR1>(x)); x = fmaxf(x, dpp<XOR2>(x)); x = fmaxf(x, dpp<XOR7>(x)); x = fmaxf(x, dpp<XOR8>(x));
  return x;
}
__device__ __forceinline__ float wave_sum(float x) { return sum_x16_x32(row_sum16(x)); }

__device__ __forceinline__ u64 gload(const GAS u64* p) { return __hip_atomic_load(p, __ATOMIC_RELAXED, __HIP_MEMORY_SCOPE_AGENT); }
__device__ __forceinline__ void gstore(GAS u64* p, unsigned tag, unsigned val) {
  __hip_atomic_store(p, ((u64)tag << 32) | (u64)val, __ATOMIC_RELAXED, __HIP_MEMORY_SCOPE_AGENT);
}
template <int N> __device__ __forceinline__ void wait_vm() { asm volatile("s_waitcnt vmcnt(%0)" ::"i"(N) : "memory"); }
__device__ __forceinline__ void wait_lds() { asm volatile("s_waitcnt lgkmcnt(0)" ::: "memory"); }
#define GLDS(src, dst) __builtin_amdgcn_global_load_lds((const GAS unsigned*)(src), (LAS unsigned*)(dst), 16, 0, 2)

__device__ __forceinline__ void ln_stats(const float (&v)[8], float& mean_b, float& den_b) {
  float s = 0.f;
#pragma unroll
  for (int i = 0; i < 8; ++i) s += v[i];
  s = wave_sum(s);
  const float mean = s * (1.f / 512.f);
  float q = 0.f;
#pragma unroll
  for (int i = 0; i < 8; ++i) { const float d = v[i] - mean; q = fmaf(d, d, q); }
  q = wave_sum(q);
  const float var = rbf(q * (1.f / 512.f));
  mean_b = rbf(mean);
  den_b = rbf(sqrtf(rbf(var + 1e-5f)));
}
__device__ __forceinline__ float ln_apply(float x, float mean_b, float den_b) { return rbf(rbf(x - mean_b) / den_b); }

__device__ __forceinline__ int slot_of(int q, int j, int kvh) { return (((q >> 4) + kvh) & 3) * 512 + j * 16 + ((q + j) & 15); }

__global__ void __launch_bounds__(THREADS, 1)
fused_decode(const u16* __restrict__ x_, const char* __restrict__ kc_, const char* __restrict__ vc_, const char* __restrict__ wq_,
             const char* __restrict__ wo_, const u16* __restrict__ w1_, const u16* __restrict__ b1_, const u16* __restrict__ w2_,
             const u16* __restrict__ b2_, unsigned* __restrict__ out_, char* ws_) {
  __shared__ __attribute__((aligned(16))) unsigned char lds[LDS_BYTES];
  LAS unsigned char* L8 = (LAS unsigned char*)lds;
  volatile LAS unsigned* FULL = (volatile LAS unsigned*)(L8 + L_FULL);
  volatile LAS unsigned* FREE = (volatile LAS unsigned*)(L8 + L_FREE);
  const GAS u16* x = (const GAS u16*)x_;
  GAS char* ws = (GAS char*)ws_;
  const int tid = threadIdx.x, lane = tid & 63, wave = __builtin_amdgcn_readfirstlane(tid >> 6);
  const int b = blockIdx.x, kvh = b >> 5, j = b & 31;
#define STAMP(i) do { } while (0)
#define FLUSH() do { } while (0)

  if (wave == 0) {
    if (lane < 48) ((LAS unsigned*)(L8 + L_FULL))[lane] = 0u;
    wait_lds();
    __builtin_amdgcn_s_barrier();
    const GAS char* kb = (const GAS char*)kc_ + (size_t)kvh * (65536 * 256);
    const GAS char* vb = (const GAS char*)vc_ + (size_t)kvh * (65536 * 256);
    const unsigned r4 = lane >> 4, c16 = lane & 15;
    const unsigned koff = r4 * 256 + 16 * (c16 ^ r4), voff = r4 * 256 + 16 * (c16 ^ (2 * r4));
    int pub = 0, s = 0, ps = 0;
    for (int q = 0; q < NQ; ++q) {
      const int slot_id = slot_of(q, j, kvh);
      if (q >= NSLOT) {
        const unsigned need = (unsigned)(q - NSLOT + 1);
        if (FREE[s] < need) {
          if (pub < q) {
            wait_vm<0>();
            for (; pub < q; ++pub) { FULL[ps] = (unsigned)(pub + 1); ps = (ps + 1 == NSLOT) ? 0 : ps + 1; }
          }
          int sp = 0;
          while (FREE[s] < need && ++sp < SPIN_LDS) __builtin_amdgcn_s_sleep(1);
        }
      }
      if (STAMPS > 9 && q == 20) STAMP(9);
      const GAS char* ks = kb + (size_t)slot_id * 8192;
      const GAS char* vs = vb + (size_t)slot_id * 8192;
#pragma unroll
      for (int i = 0; i < 8; ++i) GLDS(ks + i * 1024 + (koff ^ (64u * (i & 3))), L8 + s * SLOT_BYTES + i * 1024);
#pragma unroll
      for (int i = 0; i < 8; ++i) GLDS(vs + i * 1024 + (voff ^ (128u * (i & 1))), L8 + s * SLOT_BYTES + 8192 + i * 1024);
      if (q + 1 - pub > INFL) {
        wait_vm<INFL * 16>();
        FULL[ps] = (unsigned)(pub + 1); ps = (ps + 1 == NSLOT) ? 0 : ps + 1; ++pub;
      }
      s = (s + 1 == NSLOT) ? 0 : s + 1;
    }
    if (pub + 3 == NQ) { wait_vm<32>(); FULL[ps] = (unsigned)(pub + 1); ps = (ps + 1 == NSLOT) ? 0 : ps + 1; ++pub; }
    if (pub + 2 == NQ) { wait_vm<16>(); FULL[ps] = (unsigned)(pub + 1); ps = (ps + 1 == NSLOT) ? 0 : ps + 1; ++pub; }
    wait_vm<0>();
    for (; pub < NQ; ++pub) { FULL[ps] = (unsigned)(pub + 1); ps = (ps + 1 == NSLOT) ? 0 : ps + 1; }
    STAMP(3);
    __syncthreads();
    __syncthreads();
    FLUSH();
  } else if (wave <= NC) {
    const int cw = wave - 1;
    if (cw == 0) STAMP(0);
    const unsigned ep = __hip_atomic_load((const GAS unsigned*)(ws + W_EPOCH), __ATOMIC_RELAXED, __HIP_MEMORY_SCOPE_AGENT);
    const u32x4 xv = *(const GAS u32x4*)(x + lane * 8);
    const int cchunk = (j & 7) * 4 + (j >> 3);
    u32x4 wqv[4]; unsigned xr[4];
#pragma unroll
    for (int i = 0; i < 4; ++i) {
      const int row = cw * 128 + i * 32 + (lane >> 1);
      wqv[i] = WQ_NT ? __builtin_nontemporal_load((const GAS u32x4*)((const GAS char*)wq_ + (size_t)row * 8192 + kvh * 1024 + cchunk * 32 + (lane & 1) * 16)) : *(const GAS u32x4*)((const GAS char*)wq_ + (size_t)row * 8192 + kvh * 1024 + cchunk * 32 + (lane & 1) * 16);
      if (ABL & 1) wqv[i] = xv;
      xr[i] = x[row];
    }
    __builtin_amdgcn_s_barrier();
    const unsigned tag = __builtin_amdgcn_readfirstlane(ep) + 1u;
    {
      float v[8];
#pragma unroll
      for (int i = 0; i < 4; ++i) { v[2 * i] = lo16(xv[i]); v[2 * i + 1] = hi16(xv[i]); }
      float mb, db; ln_stats(v, mb, db);
      float acc[8];
#pragma unroll
      for (int k = 0; k < 8; ++k) acc[k] = 0.f;
#pragma unroll
      for (int i = 0; i < 4; ++i) {
        const float xn = ln_apply(bf2f(xr[i]), mb, db);
#pragma unroll
        for (int k = 0; k < 4; ++k) { acc[2 * k] = fmaf(xn, lo16(wqv[i][k]), acc[2 * k]); acc[2 * k + 1] = fmaf(xn, hi16(wqv[i][k]), acc[2 * k + 1]); }
      }
#pragma unroll
      for (int k = 0; k < 8; ++k) {
        float a = acc[k];
        a += dpp<XOR2>(a); a += dpp<0x124>(a); a += dpp<XOR8>(a);
        acc[k] = sum_x16_x32(a);
      }
      LAS float* QP = (LAS float*)(L8 + L_QP);
      if (lane < 2) {
#pragma unroll
        for (int k = 0; k < 8; ++k) QP[cw * 16 + lane * 8 + k] = acc[k];
      }
      wait_lds();
      unsigned old = 0;
      if (lane == 0) old = __hip_atomic_fetch_add((LAS unsigned*)(L8 + L_MISC), 1u, __ATOMIC_RELAXED, __HIP_MEMORY_SCOPE_WORKGROUP);
      old = __builtin_amdgcn_readfirstlane(old);
      if (old == NC - 1) {
        asm volatile("" ::: "memory");
        if (lane < 8) {
          volatile LAS float* VQ = (volatile LAS float*)QP;
          const float q0 = (VQ[2 * lane] + VQ[16 + 2 * lane]) + (VQ[32 + 2 * lane] + VQ[48 + 2 * lane]);
          const float q1 = (VQ[2 * lane + 1] + VQ[17 + 2 * lane]) + (VQ[33 + 2 * lane] + VQ[49 + 2 * lane]);
          gstore((GAS u64*)(ws + W_G0) + kvh * 256 + cchunk * 8 + lane, tag, pk2(q0, q1));
        }
        STAMP(1);
      }
    }
    const unsigned col = lane & 15, g = lane >> 4;
    const unsigned kbase = col * 256 + 16 * (g ^ col);
    const unsigned vq = (lane >> 2) & 3, vp = lane & 3;
    const unsigned vbase = 8192 + (4 * g + vq) * 256 + 32 * (4 * (g & 1) + vq) + 8 * vp;
    bf16x8 qf[4];
    f32x4 acc[8];
#pragma unroll
    for (int d = 0; d < 8; ++d) acc[d] = f32x4{0.f, 0.f, 0.f, 0.f};
    float m = -3.0e38f, lsum = 0.f;
    auto read_slot = [&](int s, unsigned need, u32x4 (&kf)[2][4], s16x4 (&vf)[8][2]) {
      asm volatile("" ::: "memory");
      const unsigned sb = (unsigned)s * SLOT_BYTES;
#pragma unroll
      for (int st = 0; st < 4; ++st) {
        const LAS unsigned char* p = L8 + ((sb + kbase) ^ (64u * st));
        kf[0][st] = *(const LAS u32x4*)p; kf[1][st] = *(const LAS u32x4*)(p + 4096);
      }
#pragma unroll
      for (int d = 0; d < 8; ++d) {
        const LAS unsigned char* p = L8 + ((sb + vbase) ^ (32u * d));
        vf[d][0] = __builtin_amdgcn_ds_read_tr16_b64_v4i16((LAS s16x4*)p);
        vf[d][1] = __builtin_amdgcn_ds_read_tr16_b64_v4i16((LAS s16x4*)(p + 4096));
      }
      wait_lds();
      FREE[s] = need;
      asm volatile("" : "+v"(kf[0][0]), "+v"(kf[0][1]), "+v"(kf[0][2]), "+v"(kf[0][3]), "+v"(kf[1][0]), "+v"(kf[1][1]), "+v"(kf[1][2]), "+v"(kf[1][3]) :: "memory");
    };
    auto compute = [&](const u32x4 (&kf)[2][4], const s16x4 (&vf)[8][2]) {
      f32x4 d0 = {0.f, 0.f, 0.f, 0.f}, d1 = {0.f, 0.f, 0.f, 0.f};
#pragma unroll
      for (int st = 0; st < 4; ++st) {
        d0 = __builtin_amdgcn_mfma_f32_16x16x32_bf16(__builtin_bit_cast(bf16x8, kf[0][st]), qf[st], d0, 0, 0, 0);
        d1 = __builtin_amdgcn_mfma_f32_16x16x32_bf16(__builtin_bit_cast(bf16x8, kf[1][st]), qf[st], d1, 0, 0, 0);
      }
      float sc[8];
      {
        const unsigned a0 = pk2(d0[0], d0[1]), a1 = pk2(d0[2], d0[3]), a2 = pk2(d1[0], d1[1]), a3 = pk2(d1[2], d1[3]);
        const unsigned c0 = pk2(lo16(a0) * SCALE, hi16(a0) * SCALE), c1 = pk2(lo16(a1) * SCALE, hi16(a1) * SCALE);
        const unsigned c2 = pk2(lo16(a2) * SCALE, hi16(a2) * SCALE), c3 = pk2(lo16(a3) * SCALE, hi16(a3) * SCALE);
        sc[0] = lo16(c0); sc[1] = hi16(c0); sc[2] = lo16(c1); sc[3] = hi16(c1);
        sc[4] = lo16(c2); sc[5] = hi16(c2); sc[6] = lo16(c3); sc[7] = hi16(c3);
      }
      float tm = fmaxf(fmaxf(fmaxf(sc[0], sc[1]), fmaxf(sc[2], sc[3])), fmaxf(fmaxf(sc[4], sc[5]), fmaxf(sc[6], sc[7])));
      tm = max_x16_x32(tm);
      if (__any(tm > m)) {
        const float mn = fmaxf(m, tm);
        const float alpha = __builtin_amdgcn_exp2f((m - mn) * LOG2E);
        lsum *= alpha;
#pragma unroll
        for (int d = 0; d < 8; ++d) acc[d] *= alpha;
        m = mn;
      }
      float p[8];
#pragma unroll
      for (int k = 0; k < 8; ++k) p[k] = __builtin_amdgcn_exp2f((sc[k] - m) * LOG2E);
      lsum += ((p[0] + p[1]) + (p[2] + p[3])) + ((p[4] + p[5]) + (p[6] + p[7]));
      const u32x4 pp = {pk2(p[0], p[1]), pk2(p[2], p[3]), pk2(p[4], p[5]), pk2(p[6], p[7])};
      const bf16x8 pf = __builtin_bit_cast(bf16x8, pp);
#pragma unroll
      for (int d = 0; d < 8; ++d) {
        const bf16x8 va = __builtin_bit_cast(bf16x8, __builtin_shufflevector(vf[d][0], vf[d][1], 0, 1, 2, 3, 4, 5, 6, 7));
        acc[d] = __builtin_amdgcn_mfma_f32_16x16x32_bf16(va, pf, acc[d], 0, 0, 0);
      }
    };
    volatile LAS unsigned* QRDY = (volatile LAS unsigned*)(L8 + L_MISC + 4);
    u32x4 kfa[2][4], kfb[2][4]; s16x4 vfa[8][2], vfb[8][2];
    static_assert(NSLOT >= 2 * NC, "first two slots of every consumer sit in the first ring lap");
    {
      int sp = 0;
      while (FULL[cw] < (unsigned)(cw + 1) && ++sp < SPIN_LDS) __builtin_amdgcn_s_sleep(1);
      read_slot(cw, cw + 1, kfa, vfa);
      sp = 0;
      while (FULL[cw + NC] < (unsigned)(cw + NC + 1) && ++sp < SPIN_LDS) __builtin_amdgcn_s_sleep(1);
      read_slot(cw + NC, cw + NC + 1, kfb, vfb);
      sp = 0;
      while (QRDY[0] == 0u && ++sp < SPIN_LDS) __builtin_amdgcn_s_sleep(1);
      asm volatile("" ::: "memory");
#pragma unroll
      for (int st = 0; st < 4; ++st) qf[st] = *(const LAS bf16x8*)(L8 + L_Q + (col & 3) * 256 + st * 64 + g * 16);
      if (cw == 0) STAMP(2);
      compute(kfa, vfa);
      compute(kfb, vfb);
    }
    u32x4 wov[4];
#pragma unroll
    for (int i = 0; i < 4; ++i) {
      const int row = (b >> 1) * 32 + i * 8 + (lane >> 3);
      wov[i] = __builtin_nontemporal_load((const GAS u32x4*)((const GAS char*)wo_ + (size_t)row * 1024 + (b & 1) * 512 + cw * 128 + (lane & 7) * 16));
      if (ABL & 2) wov[i] = xv;
    }
    int s = cw + 2 * NC; if (s >= NSLOT) s -= NSLOT;
    for (int q = cw + 2 * NC; q < NQ; q += NC) {
      const unsigned need = (unsigned)(q + 1);
      int sp = 0;
      while (FULL[s] < need && ++sp < SPIN_LDS) __builtin_amdgcn_s_sleep(1);
      read_slot(s, need, kfa, vfa);
      compute(kfa, vfa);
      s += NC; if (s >= NSLOT) s -= NSLOT;
    }
    if (cw == 0) STAMP(4);
    lsum = sum_x16_x32(lsum);
    LAS float* LO = (LAS float*)(L8 + L_O);
    LAS float* LML = (LAS float*)(L8 + L_ML);
    if (col < 4) {
#pragma unroll
      for (int d = 0; d < 8; ++d) *(LAS f32x4*)(LO + (cw * 4 + col) * 128 + 16 * d + 4 * g) = acc[d];
      if (g == 0) { LML[(cw * 4 + col) * 2] = m; LML[(cw * 4 + col) * 2 + 1] = lsum; }
    }
    __syncthreads();
    {
      const int hl = cw, h = kvh * 4 + hl;
      float mw[4], M = -3.0e38f;
#pragma unroll
      for (int w = 0; w < 4; ++w) { mw[w] = LML[(w * 4 + hl) * 2]; M = fmaxf(M, mw[w]); }
      float L = 0.f, o0 = 0.f, o1 = 0.f;
#pragma unroll
      for (int w = 0; w < 4; ++w) {
        const float wt = __builtin_amdgcn_exp2f((mw[w] - M) * LOG2E);
        L = fmaf(wt, LML[(w * 4 + hl) * 2 + 1], L);
        o0 = fmaf(wt, LO[(w * 4 + hl) * 128 + lane], o0);
        o1 = fmaf(wt, LO[(w * 4 + hl) * 128 + 64 + lane], o1);
      }
      GAS u64* g1o = (GAS u64*)(ws + W_G1O) + ((size_t)h * 32 + j) * 128;
      gstore(g1o + lane, tag, __float_as_uint(o0));
      gstore(g1o + 64 + lane, tag, __float_as_uint(o1));
      if (lane < 2) gstore((GAS u64*)(ws + W_G1ML) + (h * 2 + lane) * 32 + j, tag, __float_as_uint(lane ? L : M));
    }
    if (cw == 0) STAMP(5);
    const int h = b >> 3, dq = (b >> 1) & 3;
    LAS float* RED = (LAS float*)(L8 + L_RED);
    float Ltot;
    {
      const GAS u64* gml = (const GAS u64*)(ws + W_G1ML) + h * 64 + lane;
      const GAS u64* go = (const GAS u64*)(ws + W_G1O) + ((size_t)h * 32 + cw * 8 + (lane >> 5)) * 128 + dq * 32 + (lane & 31);
      u64 ml, v[4];
      for (int sp = 0; sp < SPIN_GLB; ++sp) {
        ml = gload(gml);
#pragma unroll
        for (int k = 0; k < 4; ++k) v[k] = gload(go + k * 256);
        bool ok = (unsigned)(ml >> 32) == tag;
#pragma unroll
        for (int k = 0; k < 4; ++k) ok &= (unsigned)(v[k] >> 32) == tag;
        if (__all(ok)) break;
        __builtin_amdgcn_s_sleep(POLL_SLEEP);
      }
      const float mlv = __uint_as_float((unsigned)ml);
      const float other = x32(mlv);
      const float ms = lane < 32 ? mlv : other, ls = lane < 32 ? other : mlv;
      float M = row_max16(ms); M = fmaxf(M, x16(M));
      const float wt = __builtin_amdgcn_exp2f((ms - M) * LOG2E);
      float Ls = row_sum16(wt * ls); Ls += x16(Ls);
      Ltot = Ls;
      float o = 0.f;
#pragma unroll
      for (int k = 0; k < 4; ++k) {
        const float w0 = __builtin_bit_cast(float, __builtin_amdgcn_readlane(__builtin_bit_cast(int, wt), cw * 8 + 2 * k));
        const float w1 = __builtin_bit_cast(float, __builtin_amdgcn_readlane(__builtin_bit_cast(int, wt), cw * 8 + 2 * k + 1));
        o = fmaf(lane < 32 ? w0 : w1, __uint_as_float((unsigned)v[k]), o);
      }
      o += x32(o);
      if (lane < 32) RED[cw * 32 + lane] = o;
    }
    if (cw == 0) STAMP(6);
    __syncthreads();
    {
      float a[8];
#pragma unroll
      for (int k = 0; k < 8; ++k) a[k] = 0.f;
#pragma unroll
      for (int i = 0; i < 4; ++i) {
        const int r = i * 8 + (lane >> 3);
        const float o = rbf(((RED[r] + RED[32 + r]) + (RED[64 + r] + RED[96 + r])) / Ltot);
#pragma unroll
        for (int k = 0; k < 4; ++k) { a[2 * k] = fmaf(o, lo16(wov[i][k]), a[2 * k]); a[2 * k + 1] = fmaf(o, hi16(wov[i][k]), a[2 * k + 1]); }
      }
      float c4[4], c2[2];
#pragma unroll
      for (int k = 0; k < 4; ++k) {
        const float keep = (lane & 32) ? a[k + 4] : a[k], send = (lane & 32) ? a[k] : a[k + 4];
        c4[k] = keep + x32(send);
      }
#pragma unroll
      for (int k = 0; k < 2; ++k) {
        const float keep = (lane & 16) ? c4[k + 2] : c4[k], send = (lane & 16) ? c4[k] : c4[k + 2];
        c2[k] = keep + x16(send);
      }
      const float keep = (lane & 8) ? c2[1] : c2[0], send = (lane & 8) ? c2[0] : c2[1];
      const float sres = keep + dpp<XOR8>(send);
      gstore((GAS u64*)(ws + W_G2) + (b >> 1) * 512 + (b & 1) * 256 + cw * 64 + (lane & 7) * 8 + (lane >> 3), tag, __float_as_uint(sres));
    }
    if (cw == 0) STAMP(7);
    FLUSH();
  } else {
    const unsigned ep = __hip_atomic_load((const GAS unsigned*)(ws + W_EPOCH), __ATOMIC_RELAXED, __HIP_MEMORY_SCOPE_AGENT);
    const u32x4 xv = *(const GAS u32x4*)(x + lane * 8);
    const int fb = b >> 1;
    const u32x2 xo = *(const GAS u32x2*)(x + 4 * fb);
    __builtin_amdgcn_s_barrier();
    const unsigned tag = __builtin_amdgcn_readfirstlane(ep) + 1u;
    {
      const GAS u64* g0 = (const GAS u64*)(ws + W_G0) + kvh * 256 + lane;
      volatile LAS unsigned* QCNT = (volatile LAS unsigned*)(L8 + L_MISC);
      { int sp = 0; while (QCNT[0] < (unsigned)NC && ++sp < SPIN_LDS) __builtin_amdgcn_s_sleep(2); }
      u64 va[4], vb[4];
      auto try_publish = [&](const u64 (&v)[4]) -> bool {
        bool ok = true;
#pragma unroll
        for (int k = 0; k < 4; ++k) { ok &= (unsigned)(v[k] >> 32) == tag; ((volatile LAS unsigned*)(L8 + L_Q))[k * 64 + lane] = (unsigned)v[k]; }
        if (!__all(ok)) return false;
        wait_lds();
        ((volatile LAS unsigned*)(L8 + L_MISC))[1] = 1u;
        asm volatile("" ::: "memory");
        return true;
      };
#pragma unroll
      for (int k = 0; k < 4; ++k) va[k] = gload(g0 + k * 64);
#pragma unroll 1
      for (int sp = 0; sp < SPIN_GLB; ++sp) {
        __builtin_amdgcn_s_sleep(QPOLL_SLEEP);
#pragma unroll
        for (int k = 0; k < 4; ++k) vb[k] = gload(g0 + k * 64);
        if (try_publish(va)) break;
        __builtin_amdgcn_s_sleep(QPOLL_SLEEP);
#pragma unroll
        for (int k = 0; k < 4; ++k) va[k] = gload(g0 + k * 64);
        if (try_publish(vb)) break;
      }
    }
    float gate[4];
    if ((b & 1) == 0 && !(ABL & 4)) {
      float v[8];
#pragma unroll
      for (int i = 0; i < 4; ++i) { v[2 * i] = lo16(xv[i]); v[2 * i + 1] = hi16(xv[i]); }
      float mb, db; ln_stats(v, mb, db);
#pragma unroll
      for (int i = 0; i < 8; ++i) v[i] = ln_apply(v[i], mb, db);
      float mb2, db2; ln_stats(v, mb2, db2);
      float hh[4], y[4];
#pragma unroll
      for (int c = 0; c < 4; ++c) { hh[c] = ln_apply(ln_apply((c & 1) ? hi16(xo[c >> 1]) : lo16(xo[c >> 1]), mb, db), mb2, db2); y[c] = 0.f; }
      const GAS u32x2* w1p = (const GAS u32x2*)w1_; const GAS u32x2* b1p = (const GAS u32x2*)b1_; const GAS u32x2* w2p = (const GAS u32x2*)w2_;
      for (int it = 0; it < 7; ++it) {
        const u32x2 a = w1p[it * 64 + lane], bb = b1p[it * 64 + lane], c = w2p[it * 64 + lane];
#pragma unroll
        for (int k = 0; k < 4; ++k) {
          const float w1v = (k & 1) ? hi16(a[k >> 1]) : lo16(a[k >> 1]);
          const float b1v = (k & 1) ? hi16(bb[k >> 1]) : lo16(bb[k >> 1]);
          const float w2v = (k & 1) ? hi16(c[k >> 1]) : lo16(c[k >> 1]);
#pragma unroll
          for (int cc = 0; cc < 4; cc += 2) {
            const unsigned t = pk2(hh[cc] * w1v, hh[cc + 1] * w1v);
            const unsigned uu = pk2(lo16(t) + b1v, hi16(t) + b1v);
            const float u0 = lo16(uu), u1 = hi16(uu);
            const float z0 = u0 * fmaf(0.044715f * u0, u0, 1.f), z1 = u1 * fmaf(0.044715f * u1, u1, 1.f);
            constexpr float CZ = -2.f * 0.7978845608028654f * LOG2E;
            const float e0 = __builtin_amdgcn_exp2f(z0 * CZ), e1 = __builtin_amdgcn_exp2f(z1 * CZ);
            const unsigned gg = pk2(u0 * __builtin_amdgcn_rcpf(1.f + e0), u1 * __builtin_amdgcn_rcpf(1.f + e1));
            y[cc] = fmaf(lo16(gg), w2v, y[cc]); y[cc + 1] = fmaf(hi16(gg), w2v, y[cc + 1]);
          }
        }
      }
      const float b2v = bf2f(b2_[0]);
#pragma unroll
      for (int c = 0; c < 4; ++c) {
        const float yy = rbf(rbf(wave_sum(y[c])) + b2v);
        gate[c] = rbf(yy / (1.f + __builtin_amdgcn_exp2f(-yy * LOG2E)));
      }
    } else {
#pragma unroll
      for (int c = 0; c < 4; ++c) gate[c] = 0.f;
    }
    float gl = (lane & 2) ? ((lane & 1) ? gate[3] : gate[2]) : ((lane & 1) ? gate[1] : gate[0]);
    float xl = (lane & 2) ? ((lane & 1) ? hi16(xo[1]) : lo16(xo[1])) : ((lane & 1) ? hi16(xo[0]) : lo16(xo[0]));
    asm volatile("" : "+v"(gl), "+v"(xl));
    __syncthreads();
    __syncthreads();
    if ((b & 1) == 0) {
      const GAS u64* g2 = (const GAS u64*)(ws + W_G2) + (lane >> 2) * 512 + 4 * fb + (lane & 3);
      u64 pv[8];
      for (int sp = 0; sp < SPIN_GLB; ++sp) {
        bool ok = true;
#pragma unroll
        for (int k = 0; k < 8; ++k) pv[k] = gload(g2 + k * 16 * 512);
#pragma unroll
        for (int k = 0; k < 8; ++k) ok &= (unsigned)(pv[k] >> 32) == tag;
        if (__all(ok)) break;
        __builtin_amdgcn_s_sleep(POLL_SLEEP);
      }
      float f[8];
#pragma unroll
      for (int k = 0; k < 8; ++k) f[k] = __uint_as_float((unsigned)pv[k]);
      float sv = ((f[0] + f[1]) + (f[2] + f[3])) + ((f[4] + f[5]) + (f[6] + f[7]));
      sv += dpp<0x124>(sv); sv += dpp<XOR8>(sv);
      sv = sum_x16_x32(sv);
      const float r = xl + rbf(rbf(sv) * gl);
      const float rn = dpp<XOR1>(r);
      if (lane == 0 || lane == 2) out_[2 * fb + (lane >> 1)] = pk2(r, rn);
      if (b == 0 && lane == 0) __hip_atomic_store((GAS unsigned*)(ws + W_EPOCH), tag, __ATOMIC_RELAXED, __HIP_MEMORY_SCOPE_AGENT);
    }
    STAMP(8);
    FLUSH();
  }
}

extern "C" void kernel_launch(void* const* d_in, const int* in_sizes, int n_in, void* d_out, int out_size, void* d_ws, size_t ws_size,
                              hipStream_t stream) {
  fused_decode<<<256, THREADS, 0, stream>>>((const u16*)d_in[0], (const char*)d_in[1], (const char*)d_in[2], (const char*)d_in[3],
                                            (const char*)d_in[4], (const u16*)d_in[6], (const u16*)d_in[7], (const u16*)d_in[8],
                                            (const u16*)d_in[9], (unsigned*)d_out, (char*)d_ws);
}
```

```cpp
#include <hip/hip_runtime.h>
#include <hip/hip_bf16.h>
#include <stdint.h>

#define STAMPS 0
#define EARLY 0
#define BLK 8
#define PHH 0
#define ALIGN_US 0
#define FC 2
#define PRE_SLEEP1 0
#define PRE_SLEEP2 0

#define LAS __attribute__((address_space(3)))
#define GAS __attribute__((address_space(1)))
typedef unsigned short u16;
typedef unsigned long long u64;
using u32x4 = __attribute__((ext_vector_type(4))) unsigned;
using u32x2 = __attribute__((ext_vector_type(2))) unsigned;
using f32x4 = __attribute__((ext_vector_type(4))) float;
using f32x2 = __attribute__((ext_vector_type(2))) float;
using bf16x2 = __attribute__((ext_vector_type(2))) __bf16;
using bf16x8 = __attribute__((ext_vector_type(8))) __bf16;
using s16x4 = __attribute__((ext_vector_type(4))) short;
typedef __amdgpu_buffer_rsrc_t rsrc_t;

constexpr int NC = 4;
constexpr int THREADS = (NC + 2) * 64;
constexpr int NSLOT = 8;
constexpr int SLOT_BYTES = 16384;
constexpr int NCH = 32;
constexpr int RING = NSLOT * SLOT_BYTES;
constexpr int L_SC = RING;
constexpr int L_FREE = L_SC + 16384;
constexpr int L_FULL = L_FREE + 128;
constexpr int L_MISC = L_FULL + 64;
constexpr int L_QP = L_MISC + 64;
constexpr int L_MX = L_QP + 256;
constexpr int L_LS = L_MX + 64;
constexpr int L_F = L_LS + 64;
constexpr int L_RED = L_F + 64;
constexpr int L_Q = L_RED + 512;
constexpr int LDS_BYTES = L_Q + 1024;
static_assert(LDS_BYTES <= 163840, "LDS");
constexpr int M_QCNT = 0, M_QRDY = 1, M_MCNT = 2, M_PCNT = 3, M_FRDY = 4;
constexpr unsigned W_EPOCH = 0;
constexpr unsigned W_G0 = 4096;
constexpr unsigned W_GM = W_G0 + 32768;
constexpr unsigned W_G2 = W_GM + 32768;
constexpr unsigned W_G1 = W_G2 + 524288;
constexpr unsigned W_END = W_G1 + 1048576;
constexpr size_t W_DBG = (size_t)256 << 20;

constexpr int SPIN_LDS = 1 << 20;
constexpr int SPIN_GLB = 1 << 17;
constexpr float LOG2E = 1.4426950408889634f;
constexpr float SCALE = 0.08838834764831845f;
constexpr float NEG_BIG = -3.0e38f;

__device__ __forceinline__ float bf2f(unsigned b) { return __uint_as_float(b << 16); }
__device__ __forceinline__ unsigned pk2(float lo, float hi) {
  f32x2 p = {lo, hi};
  return __builtin_bit_cast(unsigned, __builtin_convertvector(p, bf16x2));
}
__device__ __forceinline__ float rbf(float f) { return __uint_as_float(pk2(f, f) << 16); }
__device__ __forceinline__ float lo16(unsigned u) { return __uint_as_float(u << 16); }
__device__ __forceinline__ float hi16(unsigned u) { return __uint_as_float(u & 0xffff0000u); }
__device__ __forceinline__ float vmax(float a, float b) { return fmaxf(a, b); }
__device__ __forceinline__ float vmax3(float a, float b, float c) { return fmaxf(fmaxf(a, b), c); }

template <int CTRL> __device__ __forceinline__ float dpp(float x) {
  return __builtin_bit_cast(float, __builtin_amdgcn_mov_dpp(__builtin_bit_cast(int, x), CTRL, 0xf, 0xf, true));
}
constexpr int XOR1 = 0xB1, XOR2 = 0x4E, XOR7 = 0x141, ROR4 = 0x124, XOR8 = 0x128;
__device__ __forceinline__ float tr16(float a, float b) {
  auto s = __builtin_amdgcn_permlane16_swap(__float_as_uint(a), __float_as_uint(b), false, false);
  return __uint_as_float(s[0]) + __uint_as_float(s[1]);
}
__device__ __forceinline__ float tr32(float a, float b) {
  auto s = __builtin_amdgcn_permlane32_swap(__float_as_uint(a), __float_as_uint(b), false, false);
  return __uint_as_float(s[0]) + __uint_as_float(s[1]);
}
__device__ __forceinline__ float sum_x16_x32(float x) { x = tr16(x, x); return tr32(x, x); }
__device__ __forceinline__ float max_x16_x32(float x) {
  auto s = __builtin_amdgcn_permlane16_swap(__float_as_uint(x), __float_as_uint(x), false, false);
  x = vmax(__uint_as_float(s[0]), __uint_as_float(s[1]));
  auto t = __builtin_amdgcn_permlane32_swap(__float_as_uint(x), __float_as_uint(x), false, false);
  return vmax(__uint_as_float(t[0]), __uint_as_float(t[1]));
}
__device__ __forceinline__ float wave_sum(float x) {
  x += dpp<XOR1>(x); x += dpp<XOR2>(x); x += dpp<XOR7>(x); x += dpp<XOR8>(x);
  return sum_x16_x32(x);
}

template <int N> __device__ __forceinline__ void wait_vm() { asm volatile("s_waitcnt vmcnt(%0)" ::"i"(N) : "memory"); }
__device__ __forceinline__ void wait_lds() { asm volatile("s_waitcnt lgkmcnt(0)" ::: "memory"); }
__device__ __forceinline__ void wg_barrier() { asm volatile("s_waitcnt lgkmcnt(0)\n\ts_barrier" ::: "memory"); }
#define GLDS(src, dst) __builtin_amdgcn_global_load_lds((const GAS unsigned*)(src), (LAS unsigned*)(dst), 16, 0, 2)

__device__ __forceinline__ void gstore2(rsrc_t rs, unsigned off, unsigned tag, unsigned v0, unsigned v1) {
  const u32x4 v = {v0, tag, v1, tag};
  __builtin_amdgcn_raw_buffer_store_b128(v, rs, off, 0, 16);
}
template <int N, int SLEEP>
__device__ __forceinline__ void gather2(rsrc_t rs, const unsigned (&off)[N], unsigned tag, u32x4 (&a)[N]) {
#pragma unroll 1
  for (int sp = 0; sp < SPIN_GLB; ++sp) {
#pragma unroll
    for (int k = 0; k < N; ++k) a[k] = __builtin_amdgcn_raw_buffer_load_b128(rs, off[k], 0, 16);
    unsigned miss = 0u;
#pragma unroll
    for (int k = 0; k < N; ++k) miss |= (a[k][1] ^ tag) | (a[k][3] ^ tag);
    asm volatile("" ::: "memory");
    if (__all(miss == 0u)) break;
    if (SLEEP) __builtin_amdgcn_s_sleep(SLEEP);
  }
}

__device__ __forceinline__ void ln_stats(const float (&v)[8], float& mean_b, float& den_b) {
  float s = 0.f;
#pragma unroll
  for (int i = 0; i < 8; ++i) s += v[i];
  s = wave_sum(s);
  const float mean = s * (1.f / 512.f);
  float q = 0.f;
#pragma unroll
  for (int i = 0; i < 8; ++i) { const float d = v[i] - mean; q = fmaf(d, d, q); }
  q = wave_sum(q);
  const float var = rbf(q * (1.f / 512.f));
  mean_b = rbf(mean);
  den_b = rbf(sqrtf(rbf(var + 1e-5f)));
}
__device__ __forceinline__ float ln_apply(float x, float mean_b, float den_b) { return rbf(rbf(x - mean_b) / den_b); }

__device__ __forceinline__ int chunk_of(int i, int j, int kvh) {
  return (((i / BLK) + PHH * kvh) % (NCH / BLK)) * (32 * BLK) + j * BLK + ((i + j) % BLK);
}

__global__ void __launch_bounds__(THREADS, 1)
fused_decode(const u16* __restrict__ x_, const char* __restrict__ kc_, const char* __restrict__ vc_, const char* __restrict__ wq_,
             const char* __restrict__ wo_, const u16* __restrict__ w1_, const u16* __restrict__ b1_, const u16* __restrict__ w2_,
             const u16* __restrict__ b2_, unsigned* __restrict__ out_, char* ws_) {
  __shared__ __attribute__((aligned(16))) unsigned char lds[LDS_BYTES];
  LAS unsigned char* L8 = (LAS unsigned char*)lds;
  volatile LAS unsigned* FULL = (volatile LAS unsigned*)(L8 + L_FULL);
  volatile LAS u32x4* FREE4 = (volatile LAS u32x4*)(L8 + L_FREE);
  volatile LAS unsigned* FREE = (volatile LAS unsigned*)(L8 + L_FREE);
  volatile LAS unsigned* MISC = (volatile LAS unsigned*)(L8 + L_MISC);
  const GAS u16* x = (const GAS u16*)x_;
  GAS char* ws = (GAS char*)ws_;
  const int tid = threadIdx.x, lane = tid & 63, wave = __builtin_amdgcn_readfirstlane(tid >> 6);
  const int b = blockIdx.x, kvh = b >> 5, j = b & 31;
#define STAMP(i) do { } while (0)
#define FLUSH() do { } while (0)

  if (__builtin_expect(wave == 0, 1)) {
    STAMP(0);
    const GAS char* kb = (const GAS char*)kc_ + (size_t)kvh * (65536 * 256);
    const GAS char* vb = (const GAS char*)vc_ + (size_t)kvh * (65536 * 256);
    const unsigned r4 = lane >> 4, c16 = lane & 15;
    const unsigned koff = r4 * 256 + 16 * (c16 ^ r4), voff = r4 * 256 + 16 * (c16 ^ (2 * r4));
    int pub = 0;
#define PUBLISH1() do { FULL[pub & (NSLOT - 1)] = (unsigned)(pub + 1); ++pub; } while (0)
    if (!EARLY) {
      ((LAS unsigned*)(L8 + L_FREE))[lane] = 0u;
      wg_barrier();
    }
    for (int q = 0; q < 2 * NCH;) {
      const int s = q & (NSLOT - 1);
      if (q >= NSLOT) {
        const unsigned need = (unsigned)(q - NSLOT + 1);
        u32x4 f = FREE4[s];
        if (min(min(f[0], f[1]), min(f[2], f[3])) < need) {
          if (pub < q) { wait_vm<0>(); while (pub < q) PUBLISH1(); }
          for (int sp = 0; sp < SPIN_LDS; ++sp) {
            f = FREE4[s];
            if (min(min(f[0], f[1]), min(f[2], f[3])) >= need) break;
            __builtin_amdgcn_s_sleep(1);
          }
        }
      }
      const size_t cb = (size_t)chunk_of(q & (NCH - 1), j, kvh) * SLOT_BYTES;
      if (q < NCH) {
        const GAS char* src = kb + cb;
#pragma unroll
        for (int i = 0; i < 16; ++i) GLDS(src + i * 1024 + (koff ^ (64u * (i & 3))), L8 + s * SLOT_BYTES + i * 1024);
      } else {
        const GAS char* src = vb + cb;
#pragma unroll
        for (int i = 0; i < 16; ++i) GLDS(src + i * 1024 + (voff ^ (128u * (i & 1))), L8 + s * SLOT_BYTES + i * 1024);
      }
      ++q;
      if (EARLY && q == EARLY) {
        ((LAS unsigned*)(L8 + L_FREE))[lane] = 0u;
        wg_barrier();
      }
      if (q - pub > 3) { wait_vm<48>(); PUBLISH1(); }
    }
    if (pub == 2 * NCH - 3) { wait_vm<32>(); PUBLISH1(); }
    if (pub == 2 * NCH - 2) { wait_vm<16>(); PUBLISH1(); }
    wait_vm<0>();
    while (pub < 2 * NCH) PUBLISH1();
    STAMP(3);
    wg_barrier();
    FLUSH();
    return;
  }

  const unsigned col = lane & 15, g = lane >> 4;
  const rsrc_t rs = __builtin_amdgcn_make_buffer_rsrc((void*)ws_, (short)0, (int)W_END, 0x00020000);
  const int bx = b & 7, bi = b >> 3;
  const unsigned ep = __hip_atomic_load((const GAS unsigned*)(ws + W_EPOCH), __ATOMIC_RELAXED, __HIP_MEMORY_SCOPE_WORKGROUP);
  const u32x4 xv = *(const GAS u32x4*)(x + lane * 8);
  auto lds_arrive = [&](int word) -> unsigned {
    wait_lds();
    unsigned old = 0;
    if (lane == 0) old = __hip_atomic_fetch_add((LAS unsigned*)(L8 + L_MISC) + word, 1u, __ATOMIC_RELAXED, __HIP_MEMORY_SCOPE_WORKGROUP);
    old = __builtin_amdgcn_readfirstlane(old);
    asm volatile("" ::: "memory");
    return old;
  };
  auto lds_wait_ge = [&](volatile LAS unsigned* p, unsigned need) {
    int sp = 0;
    while (*p < need && ++sp < SPIN_LDS) __builtin_amdgcn_s_sleep(1);
    asm volatile("" ::: "memory");
  };

  if (wave <= NC) {
    const int cw = wave - 1;
    const int cchunk = (j & 7) * 4 + (j >> 3);
    u32x4 wqv[4]; unsigned xr[4];
#pragma unroll
    for (int i = 0; i < 4; ++i) {
      const int row = cw * 128 + i * 32 + (lane >> 1);
      wqv[i] = __builtin_nontemporal_load((const GAS u32x4*)((const GAS char*)wq_ + (size_t)row * 8192 + kvh * 1024 + cchunk * 32 + (lane & 1) * 16));
      xr[i] = x[row];
    }
    asm volatile("s_barrier" ::: "memory");
    const unsigned tag = __builtin_amdgcn_readfirstlane(ep) + 1u;
    {
      float v[8];
#pragma unroll
      for (int i = 0; i < 4; ++i) { v[2 * i] = lo16(xv[i]); v[2 * i + 1] = hi16(xv[i]); }
      float mb, db; ln_stats(v, mb, db);
      float a[8];
#pragma unroll
      for (int k = 0; k < 8; ++k) a[k] = 0.f;
#pragma unroll
      for (int i = 0; i < 4; ++i) {
        const float xn = ln_apply(bf2f(xr[i]), mb, db);
#pragma unroll
        for (int k = 0; k < 4; ++k) { a[2 * k] = fmaf(xn, lo16(wqv[i][k]), a[2 * k]); a[2 * k + 1] = fmaf(xn, hi16(wqv[i][k]), a[2 * k + 1]); }
      }
#pragma unroll
      for (int k = 0; k < 8; ++k) {
        float t = a[k];
        t += dpp<XOR2>(t); t += dpp<ROR4>(t); t += dpp<XOR8>(t);
        a[k] = sum_x16_x32(t);
      }
      LAS float* QP = (LAS float*)(L8 + L_QP);
      if (lane < 2) {
#pragma unroll
        for (int k = 0; k < 8; ++k) QP[cw * 16 + lane * 8 + k] = a[k];
      }
      if (lds_arrive(M_QCNT) == NC - 1) {
        if (lane < 8) {
          const float q0 = (QP[2 * lane] + QP[16 + 2 * lane]) + (QP[32 + 2 * lane] + QP[48 + 2 * lane]);
          const float q1 = (QP[2 * lane + 1] + QP[17 + 2 * lane]) + (QP[33 + 2 * lane] + QP[49 + 2 * lane]);
          gstore2(rs, W_G0 + (kvh * 32 + cchunk) * 128 + lane * 16, tag, __float_as_uint(q0), __float_as_uint(q1));
        }
        STAMP(1);
      }
    }
    const unsigned kbase = col * 256 + 16 * (g ^ col);
    bf16x8 qf[4];
    float dmax = NEG_BIG;
    auto read_k = [&](int s, u32x4 (&kf)[4][4]) {
      const unsigned sb = (unsigned)s * SLOT_BYTES;
#pragma unroll
      for (int st = 0; st < 4; ++st) {
        const LAS unsigned char* p = L8 + ((sb + kbase) ^ (64u * st));
#pragma unroll
        for (int kg = 0; kg < 4; ++kg) kf[kg][st] = *(const LAS u32x4*)(p + 4096 * kg);
      }
    };
    auto release_all = [&](int s, unsigned need) { wait_lds(); FREE4[s] = u32x4{need, need, need, need}; };
    auto scores = [&](const u32x4 (&kf)[4][4], int ch) {
      f32x4 d[4];
#pragma unroll
      for (int kg = 0; kg < 4; ++kg) d[kg] = f32x4{0.f, 0.f, 0.f, 0.f};
#pragma unroll
      for (int st = 0; st < 4; ++st)
#pragma unroll
        for (int kg = 0; kg < 4; ++kg) d[kg] = __builtin_amdgcn_mfma_f32_16x16x32_bf16(__builtin_bit_cast(bf16x8, kf[kg][st]), qf[st], d[kg], 0, 0, 0);
      unsigned c[8];
#pragma unroll
      for (int kg = 0; kg < 4; ++kg) {
        const unsigned a0 = pk2(d[kg][0], d[kg][1]), a1 = pk2(d[kg][2], d[kg][3]);
        const float t0 = lo16(a0) * SCALE, t1 = hi16(a0) * SCALE, t2 = lo16(a1) * SCALE, t3 = hi16(a1) * SCALE;
        c[2 * kg] = pk2(t0, t1); c[2 * kg + 1] = pk2(t2, t3);
        dmax = vmax3(vmax3(t0, t1, t2), t3, dmax);
      }
      if (col < 4) {
        LAS u32x4* dst = (LAS u32x4*)(L8 + L_SC + ch * 512 + (g * 4 + col) * 32);
        dst[0] = u32x4{c[0], c[1], c[2], c[3]}; dst[1] = u32x4{c[4], c[5], c[6], c[7]};
      }
    };
    const int rh = kvh * 4 + (bx >> 1), rdq = (bx & 1) * 2 + ((j >> 4) & 1), rch = (j >> 3) & 1;
    u32x4 wov[4];
    {
      u32x4 kfa[4][4], kfb[4][4];
      lds_wait_ge(FULL + cw, cw + 1);
      read_k(cw, kfa); release_all(cw, cw + 1);
      lds_wait_ge(FULL + cw + NC, cw + NC + 1);
      read_k(cw + NC, kfb); release_all(cw + NC, cw + NC + 1);
      lds_wait_ge(MISC + M_QRDY, 1u);
#pragma unroll
      for (int i = 0; i < 4; ++i) {
        const int row = (rh * 4 + rdq) * 32 + i * 8 + (lane >> 3);
        wov[i] = __builtin_nontemporal_load((const GAS u32x4*)((const GAS char*)wo_ + (size_t)row * 1024 + rch * 512 + cw * 128 + (lane & 7) * 16));
      }
#pragma unroll
      for (int st = 0; st < 4; ++st) qf[st] = *(const LAS bf16x8*)(L8 + L_Q + (col & 3) * 256 + st * 64 + g * 16);
      if (cw == 0) STAMP(2);
      scores(kfa, cw);
      scores(kfb, cw + NC);
#pragma unroll 1
      for (int q = cw + 2 * NC; q < NCH; q += NC) {
        const int s = q & (NSLOT - 1);
        lds_wait_ge(FULL + s, q + 1);
        read_k(s, kfa); release_all(s, q + 1);
        scores(kfa, q);
      }
    }
    dmax = max_x16_x32(dmax);
    if (lane < 4) ((LAS float*)(L8 + L_MX))[lane * 4 + cw] = dmax;
    lds_arrive(M_MCNT);
    lds_wait_ge(MISC + M_MCNT, NC);
    const f32x4 mx = *(const LAS f32x4*)(L8 + L_MX + (col & 3) * 16);
    const float m = rbf(vmax(vmax3(mx[0], mx[1], mx[2]), mx[3]));
    {
      float lsum = 0.f;
#pragma unroll
      for (int n = 0; n < 2; ++n) {
        LAS u32x4* rec = (LAS u32x4*)(L8 + L_SC + (cw + 4 * (g + 4 * n)) * 512 + col * 32);
#pragma unroll
        for (int t = 0; t < 2; ++t) {
          const u32x4 c = rec[t];
          u32x4 o;
#pragma unroll
          for (int k = 0; k < 4; ++k) {
            const float p0 = __builtin_amdgcn_exp2f((lo16(c[k]) - m) * LOG2E), p1 = __builtin_amdgcn_exp2f((hi16(c[k]) - m) * LOG2E);
            lsum += p0 + p1;
            o[k] = pk2(p0, p1);
          }
          rec[t] = o;
        }
      }
      lsum += dpp<ROR4>(lsum); lsum += dpp<XOR8>(lsum);
      lsum = sum_x16_x32(lsum);
      if (lane < 4) { ((LAS float*)(L8 + L_LS))[lane * 4 + cw] = lsum; if (cw == 0) ((LAS float*)(L8 + L_F))[4 + lane] = m; }
    }
    lds_arrive(M_PCNT);
    f32x2 wf[4][4];
#pragma unroll
    for (int i = 0; i < 4; ++i)
#pragma unroll
      for (int k = 0; k < 4; ++k) { wf[i][k] = f32x2{lo16(wov[i][k]), hi16(wov[i][k])}; asm volatile("" : "+v"(wf[i][k])); }
    lds_wait_ge(MISC + M_PCNT, NC);
    if (cw == 0) STAMP(4);
    const unsigned vq = (lane >> 2) & 3, vp = lane & 3;
    const unsigned vbase = (4 * g + vq) * 256 + ((32 * (4 * (g & 1) + vq)) ^ (64u * cw)) + 8 * vp;
    f32x4 acc0 = {0.f, 0.f, 0.f, 0.f}, acc1 = {0.f, 0.f, 0.f, 0.f};
    float fs = 0.f;
#pragma unroll 1
    for (int i = 0; i < NCH; ++i) {
      const int q = NCH + i, s = q & (NSLOT - 1);
      const LAS u32x4* rec = (const LAS u32x4*)(L8 + L_SC + i * 512 + (g * 4 + (col & 3)) * 32);
      const u32x4 p0 = rec[0], p1 = rec[1];
      if (i == NCH - 1) {
        lds_wait_ge(MISC + M_FRDY, 1u);
        fs = ((const LAS float*)(L8 + L_F))[col & 3];
      }
      lds_wait_ge(FULL + s, q + 1);
      const LAS unsigned char* pa = L8 + s * SLOT_BYTES + vbase;
      const LAS unsigned char* pb = L8 + s * SLOT_BYTES + (vbase ^ 32u);
      s16x4 va[4], vb[4];
#pragma unroll
      for (int t = 0; t < 4; ++t) {
        va[t] = __builtin_amdgcn_ds_read_tr16_b64_v4i16((LAS s16x4*)(pa + 4096 * t));
        vb[t] = __builtin_amdgcn_ds_read_tr16_b64_v4i16((LAS s16x4*)(pb + 4096 * t));
      }
      acc0 = __builtin_amdgcn_mfma_f32_16x16x32_bf16(__builtin_bit_cast(bf16x8, __builtin_shufflevector(va[0], va[1], 0, 1, 2, 3, 4, 5, 6, 7)), __builtin_bit_cast(bf16x8, p0), acc0, 0, 0, 0);
      acc1 = __builtin_amdgcn_mfma_f32_16x16x32_bf16(__builtin_bit_cast(bf16x8, __builtin_shufflevector(vb[0], vb[1], 0, 1, 2, 3, 4, 5, 6, 7)), __builtin_bit_cast(bf16x8, p0), acc1, 0, 0, 0);
      acc0 = __builtin_amdgcn_mfma_f32_16x16x32_bf16(__builtin_bit_cast(bf16x8, __builtin_shufflevector(va[2], va[3], 0, 1, 2, 3, 4, 5, 6, 7)), __builtin_bit_cast(bf16x8, p1), acc0, 0, 0, 0);
      acc1 = __builtin_amdgcn_mfma_f32_16x16x32_bf16(__builtin_bit_cast(bf16x8, __builtin_shufflevector(vb[2], vb[3], 0, 1, 2, 3, 4, 5, 6, 7)), __builtin_bit_cast(bf16x8, p1), acc1, 0, 0, 0);
      wait_lds();
      FREE[s * 4 + cw] = q + 1;
    }
    {
      const f32x4 a = (col & 8) ? acc1 : acc0;
      const float v0 = ((col & 4) ? a[2] : a[0]) * fs, v1 = ((col & 4) ? a[3] : a[1]) * fs;
      gstore2(rs, W_G1 + ((kvh * 4 + (col & 3)) * 32 + j) * 1024 + (32 * cw + 2 * (col & 8) + 4 * g + ((col & 4) >> 1)) * 8, tag, __float_as_uint(v0), __float_as_uint(v1));
    }
    if (cw == 0) STAMP(5);
    LAS float* RED = (LAS float*)(L8 + L_RED);
    {
      unsigned off[2]; u32x4 a[2];
#pragma unroll
      for (int k = 0; k < 2; ++k) off[k] = W_G1 + (rh * 32 + cw * 8 + 4 * k + g) * 1024 + rdq * 256 + col * 16;
      if (PRE_SLEEP1) __builtin_amdgcn_s_sleep(PRE_SLEEP1);
      gather2<2, 0>(rs, off, tag, a);
      const float o0 = __uint_as_float(a[0][0]) + __uint_as_float(a[1][0]), o1 = __uint_as_float(a[0][2]) + __uint_as_float(a[1][2]);
      const float t = tr16(o0, o1);
      const float r = tr32(t, t);
      if (lane < 32) RED[(2 * col + g) * 4 + cw] = r;
    }
    if (cw == 0) STAMP(6);
    wg_barrier();
    {
      float o[4];
#pragma unroll
      for (int i = 0; i < 4; ++i) { const f32x4 rr = *(const LAS f32x4*)(RED + (i * 8 + (lane >> 3)) * 4); o[i] = (rr[0] + rr[1]) + (rr[2] + rr[3]); }
      const unsigned p01 = pk2(o[0], o[1]), p23 = pk2(o[2], o[3]);
      o[0] = lo16(p01); o[1] = hi16(p01); o[2] = lo16(p23); o[3] = hi16(p23);
      f32x2 a[4];
#pragma unroll
      for (int k = 0; k < 4; ++k) a[k] = (wf[0][k] * o[0] + wf[1][k] * o[1]) + (wf[2][k] * o[2] + wf[3][k] * o[3]);
      const float c0 = tr32(a[0][0], a[2][0]), c1 = tr32(a[0][1], a[2][1]), c2 = tr32(a[1][0], a[3][0]), c3 = tr32(a[1][1], a[3][1]);
      float r0 = tr16(c0, c2), r1 = tr16(c1, c3);
      r0 += dpp<XOR8>(r0); r1 += dpp<XOR8>(r1);
      if (!(lane & 8)) gstore2(rs, W_G2 + ((rh * 4 + rdq) * 512 + rch * 256 + cw * 64 + (lane & 7) * 8 + g * 2) * 8, tag, __float_as_uint(r0), __float_as_uint(r1));
    }
    if (cw == 0) STAMP(7);
    FLUSH();
    return;
  }

  constexpr int SUBS = 16 / FC, LPP = FC / 2;
  const bool is_final = bi < 4 * SUBS;
  const int fb = (((bi / SUBS) * 8 + bx) * SUBS + (bi % SUBS)) & (512 / FC - 1);
  const unsigned xcol = *(const GAS unsigned*)(x + FC * fb + 2 * (lane & (LPP - 1)));
  asm volatile("s_barrier" ::: "memory");
  const unsigned tag = __builtin_amdgcn_readfirstlane(ep) + 1u;
  {
    { int sp = 0; while (MISC[M_QCNT] < (unsigned)NC && ++sp < SPIN_LDS) __builtin_amdgcn_s_sleep(2); }
    unsigned off[4]; u32x4 a[4];
#pragma unroll
    for (int k = 0; k < 4; ++k) off[k] = W_G0 + kvh * 4096 + k * 1024 + lane * 16;
    gather2<4, 4>(rs, off, tag, a);
#pragma unroll
    for (int k = 0; k < 4; ++k) ((LAS unsigned*)(L8 + L_Q))[k * 64 + lane] = pk2(__uint_as_float(a[k][0]), __uint_as_float(a[k][2]));
    wait_lds();
    MISC[M_QRDY] = 1u;
  }
  float xl0 = lo16(xcol), xl1 = hi16(xcol), gl0 = 0.f, gl1 = 0.f;
  if (is_final) {
    float v[8];
#pragma unroll
    for (int i = 0; i < 4; ++i) { v[2 * i] = lo16(xv[i]); v[2 * i + 1] = hi16(xv[i]); }
    float mb, db; ln_stats(v, mb, db);
#pragma unroll
    for (int i = 0; i < 8; ++i) v[i] = ln_apply(v[i], mb, db);
    float mb2, db2; ln_stats(v, mb2, db2);
    const float hl0 = ln_apply(ln_apply(xl0, mb, db), mb2, db2), hl1 = ln_apply(ln_apply(xl1, mb, db), mb2, db2);
    const GAS u32x2* w1p = (const GAS u32x2*)w1_; const GAS u32x2* b1p = (const GAS u32x2*)b1_; const GAS u32x2* w2p = (const GAS u32x2*)w2_;
    u32x2 wa[7], wb[7], wc[7];
#pragma unroll
    for (int it = 0; it < 7; ++it) { wa[it] = w1p[it * 64 + lane]; wb[it] = b1p[it * 64 + lane]; wc[it] = w2p[it * 64 + lane]; }
    const float b2v = bf2f(b2_[0]);
#pragma unroll 1
    for (int cc = 0; cc < LPP; ++cc) {
      const float h0 = __builtin_bit_cast(float, __builtin_amdgcn_readlane(__builtin_bit_cast(int, hl0), cc));
      const float h1 = __builtin_bit_cast(float, __builtin_amdgcn_readlane(__builtin_bit_cast(int, hl1), cc));
      float y0 = 0.f, y1 = 0.f;
#pragma unroll
      for (int it = 0; it < 7; ++it) {
#pragma unroll
        for (int k = 0; k < 4; ++k) {
          const float w1v = (k & 1) ? hi16(wa[it][k >> 1]) : lo16(wa[it][k >> 1]);
          const float b1v = (k & 1) ? hi16(wb[it][k >> 1]) : lo16(wb[it][k >> 1]);
          const float w2v = (k & 1) ? hi16(wc[it][k >> 1]) : lo16(wc[it][k >> 1]);
          const unsigned t = pk2(h0 * w1v, h1 * w1v);
          const unsigned uu = pk2(lo16(t) + b1v, hi16(t) + b1v);
          const float u0 = lo16(uu), u1 = hi16(uu);
          const float z0 = u0 * fmaf(0.044715f * u0, u0, 1.f), z1 = u1 * fmaf(0.044715f * u1, u1, 1.f);
          constexpr float CZ = -2.f * 0.7978845608028654f * LOG2E;
          const float e0 = __builtin_amdgcn_exp2f(z0 * CZ), e1 = __builtin_amdgcn_exp2f(z1 * CZ);
          const unsigned gg = pk2(u0 * __builtin_amdgcn_rcpf(1.f + e0), u1 * __builtin_amdgcn_rcpf(1.f + e1));
          y0 = fmaf(lo16(gg), w2v, y0); y1 = fmaf(hi16(gg), w2v, y1);
        }
      }
      y0 = rbf(rbf(wave_sum(y0)) + b2v); y1 = rbf(rbf(wave_sum(y1)) + b2v);
      const float g0 = rbf(y0 / (1.f + __builtin_amdgcn_exp2f(-y0 * LOG2E))), g1 = rbf(y1 / (1.f + __builtin_amdgcn_exp2f(-y1 * LOG2E)));
      gl0 = ((lane & (LPP - 1)) == cc) ? g0 : gl0; gl1 = ((lane & (LPP - 1)) == cc) ? g1 : gl1;
    }
  }
  asm volatile("" : "+v"(gl0), "+v"(xl0), "+v"(gl1), "+v"(xl1));
  {
    { int sp = 0; while (MISC[M_PCNT] < (unsigned)NC && ++sp < SPIN_LDS) __builtin_amdgcn_s_sleep(4); }
    asm volatile("" ::: "memory");
    const f32x4 ls = *(const LAS f32x4*)(L8 + L_LS + (lane & 3) * 16);
    const float mown = ((const LAS float*)(L8 + L_F))[4 + (lane & 3)];
    if (lane < 8) gstore2(rs, W_GM + b * 128 + lane * 16, tag, __float_as_uint(mown), __float_as_uint((ls[0] + ls[1]) + (ls[2] + ls[3])));
    unsigned off[2]; u32x4 a[2];
#pragma unroll
    for (int k = 0; k < 2; ++k) off[k] = W_GM + (kvh * 32 + k * 16 + (lane >> 2)) * 128 + (lane & 3) * 16;
    gather2<2, 16>(rs, off, tag, a);
    const float m0 = __uint_as_float(a[0][0]), m1 = __uint_as_float(a[1][0]);
    float M = vmax(m0, m1);
    M = vmax(M, dpp<ROR4>(M)); M = vmax(M, dpp<XOR8>(M)); M = max_x16_x32(M);
    float L = fmaf(__builtin_amdgcn_exp2f((m0 - M) * LOG2E), __uint_as_float(a[0][2]), __builtin_amdgcn_exp2f((m1 - M) * LOG2E) * __uint_as_float(a[1][2]));
    L += dpp<ROR4>(L); L += dpp<XOR8>(L); L = sum_x16_x32(L);
    if (lane < 4) ((LAS float*)(L8 + L_F))[lane] = __builtin_amdgcn_exp2f((mown - M) * LOG2E) / L;
    wait_lds();
    MISC[M_FRDY] = 1u;
  }
  wg_barrier();
  if (is_final) {
    constexpr int NL2 = FC, PPI = 64 / LPP;
    unsigned off[NL2]; u32x4 a[NL2];
#pragma unroll
    for (int k = 0; k < NL2; ++k) off[k] = W_G2 + ((k * PPI + lane / LPP) * 512 + FC * fb + 2 * (lane & (LPP - 1))) * 8;
    if (PRE_SLEEP2) __builtin_amdgcn_s_sleep(PRE_SLEEP2);
    gather2<NL2, 0>(rs, off, tag, a);
    f32x2 r[NL2];
#pragma unroll
    for (int k = 0; k < NL2; ++k) r[k] = f32x2{__uint_as_float(a[k][0]), __uint_as_float(a[k][2])};
#pragma unroll
    for (int w = 1; w < NL2; w *= 2)
#pragma unroll
      for (int k = 0; k < NL2; k += 2 * w) r[k] += r[k + w];
    float sv = tr32(r[0][0], r[0][1]);
    sv = tr16(sv, sv);
    sv += dpp<XOR8>(sv);
    if (LPP <= 4) sv += dpp<ROR4>(sv);
    if (LPP <= 2) sv += dpp<XOR2>(sv);
    if (LPP <= 1) sv += dpp<XOR1>(sv);
    const float res = (lane < 32 ? xl0 : xl1) + rbf(rbf(sv) * (lane < 32 ? gl0 : gl1));
    auto sw = __builtin_amdgcn_permlane32_swap(__float_as_uint(res), __float_as_uint(res), false, false);
    if (lane < LPP) out_[LPP * fb + lane] = pk2(__uint_as_float(sw[0]), __uint_as_float(sw[1]));
    if (b == 0 && lane == 0) __hip_atomic_store((GAS unsigned*)(ws + W_EPOCH), tag, __ATOMIC_RELAXED, __HIP_MEMORY_SCOPE_AGENT);
  }
  STAMP(8);
  FLUSH();
}

extern "C" void kernel_launch(void* const* d_in, const int* in_sizes, int n_in, void* d_out, int out_size, void* d_ws, size_t ws_size,
                              hipStream_t stream) {
  fused_decode<<<256, THREADS, 0, stream>>>((const u16*)d_in[0], (const char*)d_in[1], (const char*)d_in[2], (const char*)d_in[3],
                                            (const char*)d_in[4], (const u16*)d_in[6], (const u16*)d_in[7], (const u16*)d_in[8],
                                            (const u16*)d_in[9], (unsigned*)d_out, (char*)d_ws);
}
```

```cpp
#include <hip/hip_runtime.h>
#include <hip/hip_bf16.h>
#include <stdint.h>

#define STAMPS 0
#define EARLY 0
#define WIN 32
#define PACE 0
#define PACE0 1500
#define NPRE 2
#define WOLATE 1
#define OUT_SC1 0
#define ABL 0
#define QPRE 56
#define QSLEEP 16
#define STQ 52
#define WOEND 1
#define BLK 8
#define PHH 0
#define ALIGN_US 0
#define FC 2
#define PRE_SLEEP1 0
#define PRE_SLEEP2 0

#define LAS __attribute__((address_space(3)))
#define GAS __attribute__((address_space(1)))
typedef unsigned short u16;
typedef unsigned long long u64;
using u32x4 = __attribute__((ext_vector_type(4))) unsigned;
using u32x2 = __attribute__((ext_vector_type(2))) unsigned;
using f32x4 = __attribute__((ext_vector_type(4))) float;
using f32x2 = __attribute__((ext_vector_type(2))) float;
using bf16x2 = __attribute__((ext_vector_type(2))) __bf16;
using bf16x8 = __attribute__((ext_vector_type(8))) __bf16;
using s16x4 = __attribute__((ext_vector_type(4))) short;
typedef __amdgpu_buffer_rsrc_t rsrc_t;

constexpr int NC = 4;
constexpr int THREADS = (NC + 2) * 64;
constexpr int NSLOT = 8;
constexpr int SLOT_BYTES = 16384;
constexpr int NCH = 32;
constexpr int RING = NSLOT * SLOT_BYTES;
constexpr int L_SC = RING;
constexpr int L_FREE = L_SC + 16384;
constexpr int L_FULL = L_FREE + 128;
constexpr int L_MISC = L_FULL + 64;
constexpr int L_QP = L_MISC + 64;
constexpr int L_MX = L_QP + 256;
constexpr int L_LS = L_MX + 64;
constexpr int L_F = L_LS + 64;
constexpr int L_RED = L_F + 64;
constexpr int L_Q = L_RED + 512;
constexpr int LDS_BYTES = L_Q + 1024;
static_assert(LDS_BYTES <= 163840, "LDS");
static_assert(WIN % 4 == 0 && WIN >= 20 && WIN <= 64, "window");
constexpr int M_QCNT = 0, M_QRDY = 1, M_MCNT = 2, M_PCNT = 3, M_FRDY = 4;
constexpr unsigned W_EPOCH = 0;
constexpr unsigned W_G0 = 4096;
constexpr unsigned W_GM = W_G0 + 32768;
constexpr unsigned W_G2 = W_GM + 32768;
constexpr unsigned W_G1 = W_G2 + 524288;
constexpr unsigned W_END = W_G1 + 1048576;
constexpr size_t W_DBG = (size_t)256 << 20;

constexpr int SPIN_LDS = 1 << 20;
constexpr int SPIN_GLB = 1 << 17;
constexpr float LOG2E = 1.4426950408889634f;
constexpr float SCALE = 0.08838834764831845f;
constexpr float NEG_BIG = -3.0e38f;

__device__ __forceinline__ float bf2f(unsigned b) { return __uint_as_float(b << 16); }
__device__ __forceinline__ unsigned pk2(float lo, float hi) {
  f32x2 p = {lo, hi};
  return __builtin_bit_cast(unsigned, __builtin_convertvector(p, bf16x2));
}
__device__ __forceinline__ float rbf(float f) { return __uint_as_float(pk2(f, f) << 16); }
__device__ __forceinline__ float lo16(unsigned u) { return __uint_as_float(u << 16); }
__device__ __forceinline__ float hi16(unsigned u) { return __uint_as_float(u & 0xffff0000u); }
__device__ __forceinline__ float vmax(float a, float b) { return fmaxf(a, b); }
__device__ __forceinline__ float vmax3(float a, float b, float c) { return fmaxf(fmaxf(a, b), c); }

template <int CTRL> __device__ __forceinline__ float dpp(float x) {
  return __builtin_bit_cast(float, __builtin_amdgcn_mov_dpp(__builtin_bit_cast(int, x), CTRL, 0xf, 0xf, true));
}
constexpr int XOR1 = 0xB1, XOR2 = 0x4E, XOR7 = 0x141, ROR4 = 0x124, XOR8 = 0x128;
__device__ __forceinline__ float tr16(float a, float b) {
  auto s = __builtin_amdgcn_permlane16_swap(__float_as_uint(a), __float_as_uint(b), false, false);
  return __uint_as_float(s[0]) + __uint_as_float(s[1]);
}
__device__ __forceinline__ float tr32(float a, float b) {
  auto s = __builtin_amdgcn_permlane32_swap(__float_as_uint(a), __float_as_uint(b), false, false);
  return __uint_as_float(s[0]) + __uint_as_float(s[1]);
}
__device__ __forceinline__ float sum_x16_x32(float x) { x = tr16(x, x); return tr32(x, x); }
__device__ __forceinline__ float max_x16_x32(float x) {
  auto s = __builtin_amdgcn_permlane16_swap(__float_as_uint(x), __float_as_uint(x), false, false);
  x = vmax(__uint_as_float(s[0]), __uint_as_float(s[1]));
  auto t = __builtin_amdgcn_permlane32_swap(__float_as_uint(x), __float_as_uint(x), false, false);
  return vmax(__uint_as_float(t[0]), __uint_as_float(t[1]));
}
__device__ __forceinline__ float wave_sum(float x) {
  x += dpp<XOR1>(x); x += dpp<XOR2>(x); x += dpp<XOR7>(x); x += dpp<XOR8>(x);
  return sum_x16_x32(x);
}

template <int N> __device__ __forceinline__ void wait_vm() { asm volatile("s_waitcnt vmcnt(%0)" ::"i"(N) : "memory"); }
__device__ __forceinline__ void wait_lds() { asm volatile("s_waitcnt lgkmcnt(0)" ::: "memory"); }
__device__ __forceinline__ void wg_barrier() { asm volatile("s_waitcnt lgkmcnt(0)\n\ts_barrier" ::: "memory"); }
#define GLDS(src, dst) __builtin_amdgcn_global_load_lds((const GAS unsigned*)(src), (LAS unsigned*)(dst), 16, 0, 2)

__device__ __forceinline__ void gstore2(rsrc_t rs, unsigned off, unsigned tag, unsigned v0, unsigned v1) {
  const u32x4 v = {v0, tag, v1, tag};
  __builtin_amdgcn_raw_buffer_store_b128(v, rs, off, 0, 16);
}
template <int N, int SLEEP>
__device__ __forceinline__ void gather2(rsrc_t rs, const unsigned (&off)[N], unsigned tag, u32x4 (&a)[N]) {
#pragma unroll 1
  for (int sp = 0; sp < SPIN_GLB; ++sp) {
#pragma unroll
    for (int k = 0; k < N; ++k) a[k] = __builtin_amdgcn_raw_buffer_load_b128(rs, off[k], 0, 16);
    unsigned miss = 0u;
#pragma unroll
    for (int k = 0; k < N; ++k) miss |= (a[k][1] ^ tag) | (a[k][3] ^ tag);
    asm volatile("" ::: "memory");
    if (__all(miss == 0u)) break;
    if (SLEEP) __builtin_amdgcn_s_sleep(SLEEP);
  }
}

__device__ __forceinline__ void ln_stats(const float (&v)[8], float& mean_b, float& den_b) {
  float s = 0.f;
#pragma unroll
  for (int i = 0; i < 8; ++i) s += v[i];
  s = wave_sum(s);
  const float mean = s * (1.f / 512.f);
  float q = 0.f;
#pragma unroll
  for (int i = 0; i < 8; ++i) { const float d = v[i] - mean; q = fmaf(d, d, q); }
  q = wave_sum(q);
  const float var = rbf(q * (1.f / 512.f));
  mean_b = rbf(mean);
  den_b = rbf(sqrtf(rbf(var + 1e-5f)));
}
__device__ __forceinline__ float ln_apply(float x, float mean_b, float den_b) { return rbf(rbf(x - mean_b) / den_b); }

__device__ __forceinline__ int chunk_of(int i, int j, int kvh) {
  return (((i / BLK) + PHH * kvh) % (NCH / BLK)) * (32 * BLK) + j * BLK + ((i + j) % BLK);
}

__global__ void __launch_bounds__(THREADS, 1)
fused_decode(const char* __restrict__ kc_, char* ws_, const u16* __restrict__ x_, const char* __restrict__ wq_,
             const char* __restrict__ vc_, const char* __restrict__ wo_, unsigned* __restrict__ out_, const u16* __restrict__ w1_,
             const u16* __restrict__ b1_, const u16* __restrict__ w2_, const u16* __restrict__ b2_) {
  __shared__ __attribute__((aligned(16))) unsigned char lds[LDS_BYTES];
  LAS unsigned char* L8 = (LAS unsigned char*)lds;
  volatile LAS unsigned* FULL = (volatile LAS unsigned*)(L8 + L_FULL);
  volatile LAS u32x4* FREE4 = (volatile LAS u32x4*)(L8 + L_FREE);
  volatile LAS unsigned* FREE = (volatile LAS unsigned*)(L8 + L_FREE);
  volatile LAS unsigned* MISC = (volatile LAS unsigned*)(L8 + L_MISC);
  const GAS u16* x = (const GAS u16*)x_;
  GAS char* ws = (GAS char*)ws_;
  const int tid = threadIdx.x, lane = tid & 63, wave = __builtin_amdgcn_readfirstlane(tid >> 6);
  const int b = blockIdx.x, kvh = b >> 5, j = b & 31;
#define STAMP(i) do { } while (0)
#define FLUSH() do { } while (0)

  STAMP(0);
  if (tid < 64) ((LAS unsigned*)(L8 + L_FREE))[lane] = 0u;
  asm volatile("" :: "s"(kc_), "s"(ws_), "s"(x_), "s"(wq_));
  wg_barrier();
  const GAS char* kb = (const GAS char*)kc_ + (size_t)kvh * (65536 * 256);
  const unsigned r4 = lane >> 4, c16 = lane & 15;
  const unsigned koff = r4 * 256 + 16 * (c16 ^ r4);
  const int cchunk = (j & 7) * 4 + (j >> 3);
  u32x4 wqv[4], xv; unsigned xr[4], ep;
  if (wave == 0) {
#pragma unroll 1
    for (int u = 0; u < 4 * NPRE; ++u) {
      const GAS char* src = kb + (size_t)chunk_of(u >> 2, j, kvh) * SLOT_BYTES + (u & 3) * 4096;
      GLDS(src + koff, L8 + u * 4096);
      GLDS(src + 1024 + (koff ^ 64u), L8 + u * 4096 + 1024);
      GLDS(src + 2048 + (koff ^ 128u), L8 + u * 4096 + 2048);
      GLDS(src + 3072 + (koff ^ 192u), L8 + u * 4096 + 3072);
    }
  }
  int wave2 = wave;
  asm volatile("" : "+s"(wave2));
  if (__builtin_expect(wave2 == 0, 0)) {
    const GAS char* vb = (const GAS char*)vc_ + (size_t)kvh * (65536 * 256);
    const unsigned voff = r4 * 256 + 16 * (c16 ^ (2 * r4));
    int pub = 0;
#define PUBLISH1() do { FULL[pub & (NSLOT - 1)] = (unsigned)(pub + 1); ++pub; } while (0)
    for (int q = NPRE; q < 2 * NCH;) {
      const int s = q & (NSLOT - 1);
      if (q >= NSLOT) {
        const unsigned need = (unsigned)(q - NSLOT + 1);
        u32x4 f = FREE4[s];
        if (min(min(f[0], f[1]), min(f[2], f[3])) < need) {
          if (pub < q) { wait_vm<0>(); while (pub < q) PUBLISH1(); }
          for (int sp = 0; sp < SPIN_LDS; ++sp) {
            f = FREE4[s];
            if (min(min(f[0], f[1]), min(f[2], f[3])) >= need) break;
            __builtin_amdgcn_s_sleep(1);
          }
        }
      }
      const GAS char* src = (q < NCH ? kb : vb) + (size_t)chunk_of(q & (NCH - 1), j, kvh) * SLOT_BYTES;
      const unsigned o0 = q < NCH ? koff : voff, o1 = q < NCH ? koff ^ 64u : voff ^ 128u, o2 = q < NCH ? koff ^ 128u : voff, o3 = q < NCH ? koff ^ 192u : voff ^ 128u;
#pragma unroll
      for (int gi = 0; gi < 4; ++gi) {
        wait_vm<WIN - 4>();
        if (gi == 0 || ((4 * gi - (WIN - 4)) & 15) == 0) { while (((16 * q + 4 * gi - (WIN - 4)) >> 4) > pub) PUBLISH1(); }
        GLDS(src + gi * 4096 + o0, L8 + s * SLOT_BYTES + gi * 4096);
        GLDS(src + gi * 4096 + 1024 + o1, L8 + s * SLOT_BYTES + gi * 4096 + 1024);
        GLDS(src + gi * 4096 + 2048 + o2, L8 + s * SLOT_BYTES + gi * 4096 + 2048);
        GLDS(src + gi * 4096 + 3072 + o3, L8 + s * SLOT_BYTES + gi * 4096 + 3072);
      }
      ++q;
    }
    if (pub <= 2 * NCH - 4) { wait_vm<48>(); while (pub < 2 * NCH - 3) PUBLISH1(); }
    if (pub == 2 * NCH - 3) { wait_vm<32>(); PUBLISH1(); }
    if (pub == 2 * NCH - 2) { wait_vm<16>(); PUBLISH1(); }
    wait_vm<0>();
    while (pub < 2 * NCH) PUBLISH1();
    STAMP(3);
    wg_barrier();
    FLUSH();
    return;
  }

  if (wave <= NC && !(ABL & 1)) {
#pragma unroll
    for (int i = 0; i < 4; ++i) {
      const int row = (wave - 1) * 128 + i * 32 + (lane >> 1);
      wqv[i] = __builtin_nontemporal_load((const GAS u32x4*)((const GAS char*)wq_ + (size_t)row * 8192 + kvh * 1024 + cchunk * 32 + (lane & 1) * 16));
    }
  }
  xv = *(const GAS u32x4*)(x + lane * 8);
  if (ABL & 1) { wqv[0] = xv; wqv[1] = xv; wqv[2] = xv; wqv[3] = xv; }
  ep = __hip_atomic_load((const GAS unsigned*)(ws + W_EPOCH), __ATOMIC_RELAXED, __HIP_MEMORY_SCOPE_WORKGROUP);
#pragma unroll
  for (int i = 0; i < 4; ++i) xr[i] = *(const GAS unsigned*)(x + ((((wave - 1) & 3) * 128 + i * 32 + (lane >> 1)) & ~1));
  asm volatile("" : "+s"(wave2) :: "memory");
  const unsigned col = lane & 15, g = lane >> 4;
  const rsrc_t rs = __builtin_amdgcn_make_buffer_rsrc((void*)ws_, (short)0, (int)W_END, 0x00020000);
  const int bx = b & 7, bi = b >> 3;
  auto lds_arrive = [&](int word) __attribute__((always_inline)) -> unsigned {
    wait_lds();
    unsigned old = 0;
    if (lane == 0) old = __hip_atomic_fetch_add((LAS unsigned*)(L8 + L_MISC) + word, 1u, __ATOMIC_RELAXED, __HIP_MEMORY_SCOPE_WORKGROUP);
    old = __builtin_amdgcn_readfirstlane(old);
    asm volatile("" ::: "memory");
    return old;
  };
  auto lds_wait_ge = [&](volatile LAS unsigned* p, unsigned need) __attribute__((always_inline)) {
    int sp = 0;
    while (*p < need && ++sp < SPIN_LDS) __builtin_amdgcn_s_sleep(1);
    asm volatile("" ::: "memory");
  };

  if (wave2 <= NC) {
    const int cw = wave - 1;
    const unsigned tag = __builtin_amdgcn_readfirstlane(ep) + 1u;
    {
      float v[8];
#pragma unroll
      for (int i = 0; i < 4; ++i) { v[2 * i] = lo16(xv[i]); v[2 * i + 1] = hi16(xv[i]); }
      float mb, db; ln_stats(v, mb, db);
      float a[8];
#pragma unroll
      for (int k = 0; k < 8; ++k) a[k] = 0.f;
#pragma unroll
      for (int i = 0; i < 4; ++i) {
        const float xn = ln_apply((lane & 2) ? hi16(xr[i]) : lo16(xr[i]), mb, db);
#pragma unroll
        for (int k = 0; k < 4; ++k) { a[2 * k] = fmaf(xn, lo16(wqv[i][k]), a[2 * k]); a[2 * k + 1] = fmaf(xn, hi16(wqv[i][k]), a[2 * k + 1]); }
      }
#pragma unroll
      for (int k = 0; k < 8; ++k) {
        float t = a[k];
        t += dpp<XOR2>(t); t += dpp<ROR4>(t); t += dpp<XOR8>(t);
        a[k] = sum_x16_x32(t);
      }
      LAS float* QP = (LAS float*)(L8 + L_QP);
      if (lane < 2) {
#pragma unroll
        for (int k = 0; k < 8; ++k) QP[cw * 16 + lane * 8 + k] = a[k];
      }
      if (lds_arrive(M_QCNT) == NC - 1) {
        if (lane < 8) {
          const float q0 = (QP[2 * lane] + QP[16 + 2 * lane]) + (QP[32 + 2 * lane] + QP[48 + 2 * lane]);
          const float q1 = (QP[2 * lane + 1] + QP[17 + 2 * lane]) + (QP[33 + 2 * lane] + QP[49 + 2 * lane]);
          gstore2(rs, W_G0 + (kvh * 32 + cchunk) * 128 + lane * 16, tag, __float_as_uint(q0), __float_as_uint(q1));
        }
        STAMP(1);
      }
    }
    const unsigned kbase = col * 256 + 16 * (g ^ col);
    bf16x8 qf[4];
    float dmax = NEG_BIG;
    auto read_k = [&](int s, u32x4 (&kf)[4][4]) __attribute__((always_inline)) {
      const unsigned sb = (unsigned)s * SLOT_BYTES;
#pragma unroll
      for (int st = 0; st < 4; ++st) {
        const LAS unsigned char* p = L8 + ((sb + kbase) ^ (64u * st));
#pragma unroll
        for (int kg = 0; kg < 4; ++kg) kf[kg][st] = *(const LAS u32x4*)(p + 4096 * kg);
      }
    };
    auto release_all = [&](int s, unsigned need) __attribute__((always_inline)) { wait_lds(); FREE4[s] = u32x4{need, need, need, need}; };
    auto scores = [&](const u32x4 (&kf)[4][4], int ch) __attribute__((always_inline)) {
      f32x4 d[4];
#pragma unroll
      for (int kg = 0; kg < 4; ++kg) d[kg] = f32x4{0.f, 0.f, 0.f, 0.f};
#pragma unroll
      for (int st = 0; st < 4; ++st)
#pragma unroll
        for (int kg = 0; kg < 4; ++kg) d[kg] = __builtin_amdgcn_mfma_f32_16x16x32_bf16(__builtin_bit_cast(bf16x8, kf[kg][st]), qf[st], d[kg], 0, 0, 0);
      unsigned c[8];
#pragma unroll
      for (int kg = 0; kg < 4; ++kg) {
        const unsigned a0 = pk2(d[kg][0], d[kg][1]), a1 = pk2(d[kg][2], d[kg][3]);
        const float t0 = lo16(a0) * SCALE, t1 = hi16(a0) * SCALE, t2 = lo16(a1) * SCALE, t3 = hi16(a1) * SCALE;
        c[2 * kg] = pk2(t0, t1); c[2 * kg + 1] = pk2(t2, t3);
        dmax = vmax3(vmax3(t0, t1, t2), t3, dmax);
      }
      if (col < 4) {
        LAS u32x4* dst = (LAS u32x4*)(L8 + L_SC + ch * 512 + (g * 4 + col) * 32);
        dst[0] = u32x4{c[0], c[1], c[2], c[3]}; dst[1] = u32x4{c[4], c[5], c[6], c[7]};
      }
    };
    const int rh = kvh * 4 + (bx >> 1), rdq = (bx & 1) * 2 + ((j >> 4) & 1), rch = (j >> 3) & 1;
    u32x4 wov[4];
    auto load_wo = [&]() __attribute__((always_inline)) {
#pragma unroll
      for (int i = 0; i < 4; ++i) {
        const int row = (rh * 4 + rdq) * 32 + i * 8 + (lane >> 3);
        wov[i] = __builtin_nontemporal_load((const GAS u32x4*)((const GAS char*)wo_ + (size_t)row * 1024 + rch * 512 + cw * 128 + (lane & 7) * 16));
      }
    };
    {
      u32x4 kfa[4][4], kfb[4][4];
      lds_wait_ge(FULL + cw, cw + 1);
      read_k(cw, kfa); release_all(cw, cw + 1);
      lds_wait_ge(FULL + cw + NC, cw + NC + 1);
      read_k(cw + NC, kfb); release_all(cw + NC, cw + NC + 1);
      lds_wait_ge(MISC + M_QRDY, 1u);
      if (!WOLATE) load_wo();
#pragma unroll
      for (int st = 0; st < 4; ++st) qf[st] = *(const LAS bf16x8*)(L8 + L_Q + (col & 3) * 256 + st * 64 + g * 16);
      if (cw == 0) STAMP(2);
      scores(kfa, cw);
      scores(kfb, cw + NC);
#pragma unroll 1
      for (int q = cw + 2 * NC; q < NCH; q += NC) {
        const int s = q & (NSLOT - 1);
        lds_wait_ge(FULL + s, q + 1);
        read_k(s, kfa); release_all(s, q + 1);
        scores(kfa, q);
      }
    }
    dmax = max_x16_x32(dmax);
    if (lane < 4) ((LAS float*)(L8 + L_MX))[lane * 4 + cw] = dmax;
    lds_arrive(M_MCNT);
    lds_wait_ge(MISC + M_MCNT, NC);
    const f32x4 mx = *(const LAS f32x4*)(L8 + L_MX + (col & 3) * 16);
    const float m = rbf(vmax(vmax3(mx[0], mx[1], mx[2]), mx[3]));
    {
      float lsum = 0.f;
#pragma unroll
      for (int n = 0; n < 2; ++n) {
        LAS u32x4* rec = (LAS u32x4*)(L8 + L_SC + (cw + 4 * (g + 4 * n)) * 512 + col * 32);
#pragma unroll
        for (int t = 0; t < 2; ++t) {
          const u32x4 c = rec[t];
          u32x4 o;
#pragma unroll
          for (int k = 0; k < 4; ++k) {
            const float p0 = __builtin_amdgcn_exp2f((lo16(c[k]) - m) * LOG2E), p1 = __builtin_amdgcn_exp2f((hi16(c[k]) - m) * LOG2E);
            lsum += p0 + p1;
            o[k] = pk2(p0, p1);
          }
          rec[t] = o;
        }
      }
      lsum += dpp<ROR4>(lsum); lsum += dpp<XOR8>(lsum);
      lsum = sum_x16_x32(lsum);
      if (lane < 4) { ((LAS float*)(L8 + L_LS))[lane * 4 + cw] = lsum; if (cw == 0) ((LAS float*)(L8 + L_F))[4 + lane] = m; }
    }
    lds_arrive(M_PCNT);
    f32x2 wf[4][4];
    auto unpack_wo = [&]() __attribute__((always_inline)) {
#pragma unroll
      for (int i = 0; i < 4; ++i)
#pragma unroll
        for (int k = 0; k < 4; ++k) { wf[i][k] = f32x2{lo16(wov[i][k]), hi16(wov[i][k])}; asm volatile("" : "+v"(wf[i][k])); }
    };
    if (!WOLATE) unpack_wo();
    lds_wait_ge(MISC + M_PCNT, NC);
    if (cw == 0) STAMP(4);
    const unsigned vq = (lane >> 2) & 3, vp = lane & 3;
    const unsigned vbase = (4 * g + vq) * 256 + ((32 * (4 * (g & 1) + vq)) ^ (64u * cw)) + 8 * vp;
    f32x4 acc0 = {0.f, 0.f, 0.f, 0.f}, acc1 = {0.f, 0.f, 0.f, 0.f};
    float fs = 0.f;
#pragma unroll 1
    for (int i = 0; i < NCH; ++i) {
      const int q = NCH + i, s = q & (NSLOT - 1);
      const LAS u32x4* rec = (const LAS u32x4*)(L8 + L_SC + i * 512 + (g * 4 + (col & 3)) * 32);
      const u32x4 p0 = rec[0], p1 = rec[1];
      if (!WOEND && WOLATE && i == NCH - WOLATE) { if (ABL & 2) { wov[0] = p0; wov[1] = p0; wov[2] = p1; wov[3] = p1; } else load_wo(); }
      if (i == NCH - 1) {
        lds_wait_ge(MISC + M_FRDY, 1u);
        fs = ((const LAS float*)(L8 + L_F))[col & 3];
      }
      lds_wait_ge(FULL + s, q + 1);
      if (WOEND == 1 && i == NCH - 1) load_wo();
      const LAS unsigned char* pa = L8 + s * SLOT_BYTES + vbase;
      const LAS unsigned char* pb = L8 + s * SLOT_BYTES + (vbase ^ 32u);
      s16x4 va[4], vb[4];
#pragma unroll
      for (int t = 0; t < 4; ++t) {
        va[t] = __builtin_amdgcn_ds_read_tr16_b64_v4i16((LAS s16x4*)(pa + 4096 * t));
        vb[t] = __builtin_amdgcn_ds_read_tr16_b64_v4i16((LAS s16x4*)(pb + 4096 * t));
      }
      acc0 = __builtin_amdgcn_mfma_f32_16x16x32_bf16(__builtin_bit_cast(bf16x8, __builtin_shufflevector(va[0], va[1], 0, 1, 2, 3, 4, 5, 6, 7)), __builtin_bit_cast(bf16x8, p0), acc0, 0, 0, 0);
      acc1 = __builtin_amdgcn_mfma_f32_16x16x32_bf16(__builtin_bit_cast(bf16x8, __builtin_shufflevector(vb[0], vb[1], 0, 1, 2, 3, 4, 5, 6, 7)), __builtin_bit_cast(bf16x8, p0), acc1, 0, 0, 0);
      acc0 = __builtin_amdgcn_mfma_f32_16x16x32_bf16(__builtin_bit_cast(bf16x8, __builtin_shufflevector(va[2], va[3], 0, 1, 2, 3, 4, 5, 6, 7)), __builtin_bit_cast(bf16x8, p1), acc0, 0, 0, 0);
      acc1 = __builtin_amdgcn_mfma_f32_16x16x32_bf16(__builtin_bit_cast(bf16x8, __builtin_shufflevector(vb[2], vb[3], 0, 1, 2, 3, 4, 5, 6, 7)), __builtin_bit_cast(bf16x8, p1), acc1, 0, 0, 0);
      wait_lds();
      FREE[s * 4 + cw] = q + 1;
    }
    {
      const f32x4 a = (col & 8) ? acc1 : acc0;
      const float v0 = ((col & 4) ? a[2] : a[0]) * fs, v1 = ((col & 4) ? a[3] : a[1]) * fs;
      gstore2(rs, W_G1 + ((kvh * 4 + (col & 3)) * 32 + j) * 1024 + (32 * cw + 2 * (col & 8) + 4 * g + ((col & 4) >> 1)) * 8, tag, __float_as_uint(v0), __float_as_uint(v1));
    }
    if (WOEND == 2) load_wo();
    if (cw == 0) STAMP(5);
    LAS float* RED = (LAS float*)(L8 + L_RED);
    {
      unsigned off[2]; u32x4 a[2];
#pragma unroll
      for (int k = 0; k < 2; ++k) off[k] = W_G1 + (rh * 32 + cw * 8 + 4 * k + g) * 1024 + rdq * 256 + col * 16;
      if (WOLATE) unpack_wo();
      if (PRE_SLEEP1) __builtin_amdgcn_s_sleep(PRE_SLEEP1);
      gather2<2, 0>(rs, off, tag, a);
      const float o0 = __uint_as_float(a[0][0]) + __uint_as_float(a[1][0]), o1 = __uint_as_float(a[0][2]) + __uint_as_float(a[1][2]);
      const float t = tr16(o0, o1);
      const float r = tr32(t, t);
      if (lane < 32) RED[(2 * col + g) * 4 + cw] = r;
    }
    if (cw == 0) STAMP(6);
    wg_barrier();
    {
      float o[4];
#pragma unroll
      for (int i = 0; i < 4; ++i) { const f32x4 rr = *(const LAS f32x4*)(RED + (i * 8 + (lane >> 3)) * 4); o[i] = (rr[0] + rr[1]) + (rr[2] + rr[3]); }
      const unsigned p01 = pk2(o[0], o[1]), p23 = pk2(o[2], o[3]);
      o[0] = lo16(p01); o[1] = hi16(p01); o[2] = lo16(p23); o[3] = hi16(p23);
      f32x2 a[4];
#pragma unroll
      for (int k = 0; k < 4; ++k) a[k] = (wf[0][k] * o[0] + wf[1][k] * o[1]) + (wf[2][k] * o[2] + wf[3][k] * o[3]);
      const float c0 = tr32(a[0][0], a[2][0]), c1 = tr32(a[0][1], a[2][1]), c2 = tr32(a[1][0], a[3][0]), c3 = tr32(a[1][1], a[3][1]);
      float r0 = tr16(c0, c2), r1 = tr16(c1, c3);
      r0 += dpp<XOR8>(r0); r1 += dpp<XOR8>(r1);
      if (!(lane & 8)) gstore2(rs, W_G2 + ((rh * 4 + rdq) * 512 + rch * 256 + cw * 64 + (lane & 7) * 8 + g * 2) * 8, tag, __float_as_uint(r0), __float_as_uint(r1));
    }
    if (cw == 0) STAMP(7);
    FLUSH();
  } else {

  constexpr int SUBS = 16 / FC, LPP = FC / 2;
  const bool is_final = bi < 4 * SUBS;
  const int fb = (((bi / SUBS) * 8 + bx) * SUBS + (bi % SUBS)) & (512 / FC - 1);
  const unsigned xcol = *(const GAS unsigned*)(x + FC * fb + 2 * (lane & (LPP - 1)));
  const unsigned tag = __builtin_amdgcn_readfirstlane(ep) + 1u;
  {
    { int sp = 0; while (MISC[M_QCNT] < (unsigned)NC && ++sp < SPIN_LDS) __builtin_amdgcn_s_sleep(2); }
    unsigned off[4]; u32x4 a[4];
#pragma unroll
    for (int k = 0; k < 4; ++k) off[k] = W_G0 + kvh * 4096 + k * 1024 + lane * 16;
    if (QPRE) __builtin_amdgcn_s_sleep(QPRE);
    if (!(ABL & 8)) gather2<4, QSLEEP>(rs, off, tag, a); else { a[0] = xv; a[1] = xv; a[2] = xv; a[3] = xv; }
#pragma unroll
    for (int k = 0; k < 4; ++k) ((LAS unsigned*)(L8 + L_Q))[k * 64 + lane] = pk2(__uint_as_float(a[k][0]), __uint_as_float(a[k][2]));
    wait_lds();
    MISC[M_QRDY] = 1u;
  }
  float xl0 = lo16(xcol), xl1 = hi16(xcol), gl0 = 0.f, gl1 = 0.f;
  if (is_final && !(ABL & 4)) {
    float v[8];
#pragma unroll
    for (int i = 0; i < 4; ++i) { v[2 * i] = lo16(xv[i]); v[2 * i + 1] = hi16(xv[i]); }
    float mb, db; ln_stats(v, mb, db);
#pragma unroll
    for (int i = 0; i < 8; ++i) v[i] = ln_apply(v[i], mb, db);
    float mb2, db2; ln_stats(v, mb2, db2);
    const float hl0 = ln_apply(ln_apply(xl0, mb, db), mb2, db2), hl1 = ln_apply(ln_apply(xl1, mb, db), mb2, db2);
    const GAS u32x2* w1p = (const GAS u32x2*)w1_; const GAS u32x2* b1p = (const GAS u32x2*)b1_; const GAS u32x2* w2p = (const GAS u32x2*)w2_;
    u32x2 wa[7], wb[7], wc[7];
#pragma unroll
    for (int it = 0; it < 7; ++it) { wa[it] = w1p[it * 64 + lane]; wb[it] = b1p[it * 64 + lane]; wc[it] = w2p[it * 64 + lane]; }
    const float b2v = bf2f(b2_[0]);
#pragma unroll 1
    for (int cc = 0; cc < LPP; ++cc) {
      const float h0 = __builtin_bit_cast(float, __builtin_amdgcn_readlane(__builtin_bit_cast(int, hl0), cc));
      const float h1 = __builtin_bit_cast(float, __builtin_amdgcn_readlane(__builtin_bit_cast(int, hl1), cc));
      float y0 = 0.f, y1 = 0.f;
#pragma unroll
      for (int it = 0; it < 7; ++it) {
#pragma unroll
        for (int k = 0; k < 4; ++k) {
          const float w1v = (k & 1) ? hi16(wa[it][k >> 1]) : lo16(wa[it][k >> 1]);
          const float b1v = (k & 1) ? hi16(wb[it][k >> 1]) : lo16(wb[it][k >> 1]);
          const float w2v = (k & 1) ? hi16(wc[it][k >> 1]) : lo16(wc[it][k >> 1]);
          const unsigned t = pk2(h0 * w1v, h1 * w1v);
          const unsigned uu = pk2(lo16(t) + b1v, hi16(t) + b1v);
          const float u0 = lo16(uu), u1 = hi16(uu);
          const float z0 = u0 * fmaf(0.044715f * u0, u0, 1.f), z1 = u1 * fmaf(0.044715f * u1, u1, 1.f);
          constexpr float CZ = -2.f * 0.7978845608028654f * LOG2E;
          const float e0 = __builtin_amdgcn_exp2f(z0 * CZ), e1 = __builtin_amdgcn_exp2f(z1 * CZ);
          const unsigned gg = pk2(u0 * __builtin_amdgcn_rcpf(1.f + e0), u1 * __builtin_amdgcn_rcpf(1.f + e1));
          y0 = fmaf(lo16(gg), w2v, y0); y1 = fmaf(hi16(gg), w2v, y1);
        }
      }
      y0 = rbf(rbf(wave_sum(y0)) + b2v); y1 = rbf(rbf(wave_sum(y1)) + b2v);
      const float g0 = rbf(y0 / (1.f + __builtin_amdgcn_exp2f(-y0 * LOG2E))), g1 = rbf(y1 / (1.f + __builtin_amdgcn_exp2f(-y1 * LOG2E)));
      gl0 = ((lane & (LPP - 1)) == cc) ? g0 : gl0; gl1 = ((lane & (LPP - 1)) == cc) ? g1 : gl1;
    }
  }
  asm volatile("" : "+v"(gl0), "+v"(xl0), "+v"(gl1), "+v"(xl1));
  {
    { int sp = 0; while (MISC[M_PCNT] < (unsigned)NC && ++sp < SPIN_LDS) __builtin_amdgcn_s_sleep(4); }
    asm volatile("" ::: "memory");
    const f32x4 ls = *(const LAS f32x4*)(L8 + L_LS + (lane & 3) * 16);
    const float mown = ((const LAS float*)(L8 + L_F))[4 + (lane & 3)];
    if (lane < 8) gstore2(rs, W_GM + b * 128 + lane * 16, tag, __float_as_uint(mown), __float_as_uint((ls[0] + ls[1]) + (ls[2] + ls[3])));
    unsigned off[2]; u32x4 a[2];
#pragma unroll
    for (int k = 0; k < 2; ++k) off[k] = W_GM + (kvh * 32 + k * 16 + (lane >> 2)) * 128 + (lane & 3) * 16;
    if (STQ) { int sp = 0; while (FULL[STQ & (NSLOT - 1)] < (unsigned)(STQ + 1) && ++sp < SPIN_LDS) __builtin_amdgcn_s_sleep(8); }
    if (!(ABL & 16)) gather2<2, 16>(rs, off, tag, a); else { a[0] = xv; a[1] = xv; }
    const float m0 = __uint_as_float(a[0][0]), m1 = __uint_as_float(a[1][0]);
    float M = vmax(m0, m1);
    M = vmax(M, dpp<ROR4>(M)); M = vmax(M, dpp<XOR8>(M)); M = max_x16_x32(M);
    float L = fmaf(__builtin_amdgcn_exp2f((m0 - M) * LOG2E), __uint_as_float(a[0][2]), __builtin_amdgcn_exp2f((m1 - M) * LOG2E) * __uint_as_float(a[1][2]));
    L += dpp<ROR4>(L); L += dpp<XOR8>(L); L = sum_x16_x32(L);
    if (lane < 4) ((LAS float*)(L8 + L_F))[lane] = __builtin_amdgcn_exp2f((mown - M) * LOG2E) / L;
    wait_lds();
    MISC[M_FRDY] = 1u;
  }
  wg_barrier();
  if (is_final) {
    constexpr int NL2 = FC, PPI = 64 / LPP;
    unsigned off[NL2]; u32x4 a[NL2];
#pragma unroll
    for (int k = 0; k < NL2; ++k) off[k] = W_G2 + ((k * PPI + lane / LPP) * 512 + FC * fb + 2 * (lane & (LPP - 1))) * 8;
    if (PRE_SLEEP2) __builtin_amdgcn_s_sleep(PRE_SLEEP2);
    gather2<NL2, 0>(rs, off, tag, a);
    f32x2 r[NL2];
#pragma unroll
    for (int k = 0; k < NL2; ++k) r[k] = f32x2{__uint_as_float(a[k][0]), __uint_as_float(a[k][2])};
#pragma unroll
    for (int w = 1; w < NL2; w *= 2)
#pragma unroll
      for (int k = 0; k < NL2; k += 2 * w) r[k] += r[k + w];
    float sv = tr32(r[0][0], r[0][1]);
    sv = tr16(sv, sv);
    sv += dpp<XOR8>(sv);
    if (LPP <= 4) sv += dpp<ROR4>(sv);
    if (LPP <= 2) sv += dpp<XOR2>(sv);
    if (LPP <= 1) sv += dpp<XOR1>(sv);
    const float res = (lane < 32 ? xl0 : xl1) + rbf(rbf(sv) * (lane < 32 ? gl0 : gl1));
    auto sw = __builtin_amdgcn_permlane32_swap(__float_as_uint(res), __float_as_uint(res), false, false);
    if (lane < LPP) {
      const unsigned res2 = pk2(__uint_as_float(sw[0]), __uint_as_float(sw[1]));
      if (OUT_SC1) __hip_atomic_store((GAS unsigned*)out_ + LPP * fb + lane, res2, __ATOMIC_RELAXED, __HIP_MEMORY_SCOPE_AGENT);
      else out_[LPP * fb + lane] = res2;
    }
    if (b == 0 && lane == 0) __hip_atomic_store((GAS unsigned*)(ws + W_EPOCH), tag, __ATOMIC_RELAXED, __HIP_MEMORY_SCOPE_AGENT);
  }
  STAMP(8);
  FLUSH();
  }
  __builtin_amdgcn_s_waitcnt(0x0F70);
}

extern "C" void kernel_launch(void* const* d_in, const int* in_sizes, int n_in, void* d_out, int out_size, void* d_ws, size_t ws_size,
                              hipStream_t stream) {
  fused_decode<<<256, THREADS, 0, stream>>>((const char*)d_in[1], (char*)d_ws, (const u16*)d_in[0], (const char*)d_in[3],
                                            (const char*)d_in[2], (const char*)d_in[4], (unsigned*)d_out, (const u16*)d_in[6],
                                            (const u16*)d_in[7], (const u16*)d_in[8], (const u16*)d_in[9]);
}
```

```cpp
#include <hip/hip_runtime.h>
#include <hip/hip_bf16.h>
#include <stdint.h>

#define STAMPS 0
#define WIN 10
#define GS 1
#define NPRE 2
#define QPRE 56
#define STQ 52
#define FC 2

#define LAS __attribute__((address_space(3)))
#define GAS __attribute__((address_space(1)))
#define INL __attribute__((always_inline))
typedef unsigned short u16;
typedef unsigned long long u64;
using u32x4 = __attribute__((ext_vector_type(4))) unsigned;
using u32x2 = __attribute__((ext_vector_type(2))) unsigned;
using f32x4 = __attribute__((ext_vector_type(4))) float;
using f32x2 = __attribute__((ext_vector_type(2))) float;
using bf16x2 = __attribute__((ext_vector_type(2))) __bf16;
using bf16x8 = __attribute__((ext_vector_type(8))) __bf16;
using s16x4 = __attribute__((ext_vector_type(4))) short;
typedef __amdgpu_buffer_rsrc_t rsrc_t;

constexpr int NL = 2, NC = 4;
constexpr int THREADS = (NL + NC + 1) * 64;
constexpr int NSLOT = 8;
constexpr int SLOT_BYTES = 16384;
constexpr int NCH = 32;
constexpr int RING = NSLOT * SLOT_BYTES;
constexpr int L_SC = RING;
constexpr int L_FREE = L_SC + 16384;
constexpr int L_FULL = L_FREE + 128;
constexpr int L_MISC = L_FULL + 64;
constexpr int L_QP = L_MISC + 64;
constexpr int L_MX = L_QP + 256;
constexpr int L_LS = L_MX + 64;
constexpr int L_F = L_LS + 64;
constexpr int L_RED = L_F + 64;
constexpr int L_Q = L_RED + 512;
constexpr int LDS_BYTES = L_Q + 1024;
static_assert(LDS_BYTES <= 163840, "LDS");
static_assert(WIN % GS == 0 && 8 % GS == 0 && WIN >= 6 && WIN <= 32 && NPRE >= 1 && NPRE <= 4, "window");
constexpr int M_QCNT = 0, M_QRDY = 1, M_MCNT = 2, M_PCNT = 3, M_FRDY = 4;
constexpr unsigned W_EPOCH = 0;
constexpr unsigned W_G0 = 4096;
constexpr unsigned W_GM = W_G0 + 32768;
constexpr unsigned W_G2 = W_GM + 32768;
constexpr unsigned W_G1 = W_G2 + 524288;
constexpr unsigned W_END = W_G1 + 1048576;

constexpr int SPIN_LDS = 1 << 20;
constexpr int SPIN_GLB = 1 << 17;
constexpr float LOG2E = 1.4426950408889634f;
constexpr float SCALE = 0.08838834764831845f;
constexpr float NEG_BIG = -3.0e38f;

__device__ __forceinline__ float bf2f(unsigned b) { return __uint_as_float(b << 16); }
__device__ __forceinline__ unsigned pk2(float lo, float hi) {
  f32x2 p = {lo, hi};
  return __builtin_bit_cast(unsigned, __builtin_convertvector(p, bf16x2));
}
__device__ __forceinline__ float rbf(float f) { return __uint_as_float(pk2(f, f) << 16); }
__device__ __forceinline__ float lo16(unsigned u) { return __uint_as_float(u << 16); }
__device__ __forceinline__ float hi16(unsigned u) { return __uint_as_float(u & 0xffff0000u); }

template <int CTRL> __device__ __forceinline__ float dpp(float x) {
  return __builtin_bit_cast(float, __builtin_amdgcn_mov_dpp(__builtin_bit_cast(int, x), CTRL, 0xf, 0xf, true));
}
constexpr int XOR1 = 0xB1, XOR2 = 0x4E, XOR7 = 0x141, ROR4 = 0x124, XOR8 = 0x128;
__device__ __forceinline__ float tr16(float a, float b) {
  auto s = __builtin_amdgcn_permlane16_swap(__float_as_uint(a), __float_as_uint(b), false, false);
  return __uint_as_float(s[0]) + __uint_as_float(s[1]);
}
__device__ __forceinline__ float tr32(float a, float b) {
  auto s = __builtin_amdgcn_permlane32_swap(__float_as_uint(a), __float_as_uint(b), false, false);
  return __uint_as_float(s[0]) + __uint_as_float(s[1]);
}
__device__ __forceinline__ float sum_x16_x32(float x) { x = tr16(x, x); return tr32(x, x); }
__device__ __forceinline__ float max_x16_x32(float x) {
  auto s = __builtin_amdgcn_permlane16_swap(__float_as_uint(x), __float_as_uint(x), false, false);
  x = fmaxf(__uint_as_float(s[0]), __uint_as_float(s[1]));
  auto t = __builtin_amdgcn_permlane32_swap(__float_as_uint(x), __float_as_uint(x), false, false);
  return fmaxf(__uint_as_float(t[0]), __uint_as_float(t[1]));
}
__device__ __forceinline__ float wave_sum(float x) {
  x += dpp<XOR1>(x); x += dpp<XOR2>(x); x += dpp<XOR7>(x); x += dpp<XOR8>(x);
  return sum_x16_x32(x);
}

template <int N> __device__ __forceinline__ void wait_vm() { asm volatile("s_waitcnt vmcnt(%0)" ::"i"(N) : "memory"); }
__device__ __forceinline__ void wait_lds() { asm volatile("s_waitcnt lgkmcnt(0)" ::: "memory"); }
__device__ __forceinline__ void wg_barrier() { asm volatile("s_waitcnt lgkmcnt(0)\n\ts_barrier" ::: "memory"); }
#define GLDS(src, dst) __builtin_amdgcn_global_load_lds((const GAS unsigned*)(src), (LAS unsigned*)(dst), 16, 0, 2)

__device__ __forceinline__ void gstore2(rsrc_t rs, unsigned off, unsigned tag, unsigned v0, unsigned v1) {
  const u32x4 v = {v0, tag, v1, tag};
  __builtin_amdgcn_raw_buffer_store_b128(v, rs, off, 0, 16);
}
template <int N, int SLEEP>
__device__ __forceinline__ void gather2(rsrc_t rs, const unsigned (&off)[N], unsigned tag, u32x4 (&a)[N]) {
#pragma unroll 1
  for (int sp = 0; sp < SPIN_GLB; ++sp) {
#pragma unroll
    for (int k = 0; k < N; ++k) a[k] = __builtin_amdgcn_raw_buffer_load_b128(rs, off[k], 0, 16);
    unsigned miss = 0u;
#pragma unroll
    for (int k = 0; k < N; ++k) miss |= (a[k][1] ^ tag) | (a[k][3] ^ tag);
    asm volatile("" ::: "memory");
    if (__all(miss == 0u)) break;
    if (SLEEP) __builtin_amdgcn_s_sleep(SLEEP);
  }
}

__device__ __forceinline__ void ln_stats(const float (&v)[8], float& mean_b, float& den_b) {
  float s = 0.f;
#pragma unroll
  for (int i = 0; i < 8; ++i) s += v[i];
  s = wave_sum(s);
  const float mean = s * (1.f / 512.f);
  float q = 0.f;
#pragma unroll
  for (int i = 0; i < 8; ++i) { const float d = v[i] - mean; q = fmaf(d, d, q); }
  q = wave_sum(q);
  const float var = rbf(q * (1.f / 512.f));
  mean_b = rbf(mean);
  den_b = rbf(sqrtf(rbf(var + 1e-5f)));
}
__device__ __forceinline__ float ln_apply(float x, float mean_b, float den_b) { return rbf(rbf(x - mean_b) / den_b); }

__device__ __forceinline__ int chunk_of(int i, int j) { return (i >> 3) * 256 + j * 8 + ((i + j) & 7); }

__global__ void __launch_bounds__(THREADS, 1)
fused_decode(const char* __restrict__ kc_, char* ws_, const u16* __restrict__ x_, const char* __restrict__ wq_,
             const char* __restrict__ vc_, const char* __restrict__ wo_, unsigned* __restrict__ out_, const u16* __restrict__ w1_,
             const u16* __restrict__ b1_, const u16* __restrict__ w2_, const u16* __restrict__ b2_) {
  __shared__ __attribute__((aligned(16))) unsigned char lds[LDS_BYTES];
  LAS unsigned char* L8 = (LAS unsigned char*)lds;
  volatile LAS unsigned* FULL = (volatile LAS unsigned*)(L8 + L_FULL);
  volatile LAS u32x2* FULL2 = (volatile LAS u32x2*)(L8 + L_FULL);
  volatile LAS u32x4* FREE4 = (volatile LAS u32x4*)(L8 + L_FREE);
  volatile LAS unsigned* FREE = (volatile LAS unsigned*)(L8 + L_FREE);
  volatile LAS unsigned* MISC = (volatile LAS unsigned*)(L8 + L_MISC);
  const GAS u16* x = (const GAS u16*)x_;
  GAS char* ws = (GAS char*)ws_;
  const int tid = threadIdx.x, lane = tid & 63, wave = __builtin_amdgcn_readfirstlane(tid >> 6);
  const int b = blockIdx.x, kvh = b >> 5, j = b & 31;
#define STAMP(i) do { } while (0)
#define FLUSH() do { } while (0)

  if (wave == 0) STAMP(0);
  if (tid < 64) ((LAS unsigned*)(L8 + L_FREE))[lane] = 0u;
  asm volatile("" :: "s"(kc_), "s"(ws_), "s"(x_), "s"(wq_));
  wg_barrier();
  const GAS char* kb = (const GAS char*)kc_ + (size_t)kvh * (65536 * 256) + wave * 8192;
  const unsigned r4 = lane >> 4, c16 = lane & 15;
  const unsigned koff = r4 * 256 + 16 * (c16 ^ r4);
  if (wave < NL) {
#pragma unroll 1
    for (int u = 0; u < 2 * NPRE; ++u) {
      const GAS char* src = kb + (size_t)chunk_of(u >> 1, j) * SLOT_BYTES + (u & 1) * 4096;
      LAS unsigned char* dst = L8 + (u >> 1) * SLOT_BYTES + wave * 8192 + (u & 1) * 4096;
      GLDS(src + koff, dst);
      GLDS(src + 1024 + (koff ^ 64u), dst + 1024);
      GLDS(src + 2048 + (koff ^ 128u), dst + 2048);
      GLDS(src + 3072 + (koff ^ 192u), dst + 3072);
    }
  }
  int role = wave;
  asm volatile("" : "+s"(role));
  if (__builtin_expect(role < NL, 0)) {
    int pub = 0;
    auto publish = [&]() INL { FULL[(pub & (NSLOT - 1)) * 2 + wave] = (unsigned)(pub + 1); ++pub; };
    auto run = [&](int q0, int q1, const GAS char* base, unsigned o0, unsigned o1, unsigned o2, unsigned o3) INL {
#pragma unroll 1
      for (int q = q0; q < q1; ++q) {
        const int s = q & (NSLOT - 1);
        if (q >= NSLOT) {
          const unsigned need = (unsigned)(q - NSLOT + 1);
          u32x4 f = FREE4[s];
          if (min(min(f[0], f[1]), min(f[2], f[3])) < need) {
            if (pub < q) { wait_vm<0>(); while (pub < q) publish(); }
            for (int sp = 0; sp < SPIN_LDS; ++sp) {
              f = FREE4[s];
              if (min(min(f[0], f[1]), min(f[2], f[3])) >= need) break;
              __builtin_amdgcn_s_sleep(1);
            }
          }
        }
        const GAS char* src = base + (size_t)chunk_of(q & (NCH - 1), j) * SLOT_BYTES;
        LAS unsigned char* dst = L8 + s * SLOT_BYTES + wave * 8192;
#pragma unroll
        for (int i = 0; i < 8; ++i) {
          if (i % GS == 0) {
            wait_vm<WIN - GS>();
            if (((i - (WIN - GS)) & 7) == 0) { while (((8 * q + i - (WIN - GS)) >> 3) > pub) publish(); }
          }
          GLDS(src + i * 1024 + ((i & 3) == 0 ? o0 : (i & 3) == 1 ? o1 : (i & 3) == 2 ? o2 : o3), dst + i * 1024);
        }
      }
    };
    run(NPRE, NCH, kb, koff, koff ^ 64u, koff ^ 128u, koff ^ 192u);
    const GAS char* vb = (const GAS char*)vc_ + (size_t)kvh * (65536 * 256) + wave * 8192;
    const unsigned voff = r4 * 256 + 16 * (c16 ^ (2 * r4));
    run(NCH, 2 * NCH, vb, voff, voff ^ 128u, voff, voff ^ 128u);
    if (pub <= 2 * NCH - 3) { wait_vm<16>(); while (pub < 2 * NCH - 2) publish(); }
    if (pub == 2 * NCH - 2) { wait_vm<8>(); publish(); }
    wait_vm<0>();
    while (pub < 2 * NCH) publish();
    if (wave == 0) STAMP(3);
    wg_barrier();
    FLUSH();
    return;
  }

  const int cw = (wave - NL) & 3;
  const int qchunk = kvh * 32 + (j & 7) * 4 + (j >> 3);
  u32x4 wqv[4]; unsigned xr[4];
  const u32x4 xv = *(const GAS u32x4*)(x + lane * 8);
  const unsigned ep = __hip_atomic_load((const GAS unsigned*)(ws + W_EPOCH), __ATOMIC_RELAXED, __HIP_MEMORY_SCOPE_WORKGROUP);
#pragma unroll
  for (int i = 0; i < 4; ++i) xr[i] = *(const GAS unsigned*)(x + ((cw * 128 + i * 32 + (lane >> 1)) & ~1));
  if (wave < NL + NC) {
#pragma unroll
    for (int i = 0; i < 4; ++i) {
      const int row = cw * 128 + i * 32 + (lane >> 1);
      wqv[i] = __builtin_nontemporal_load((const GAS u32x4*)((const GAS char*)wq_ + (size_t)row * 8192 + qchunk * 32 + (lane & 1) * 16));
    }
  }
  asm volatile("" : "+s"(role) :: "memory");
  const unsigned col = lane & 15, g = lane >> 4;
  const rsrc_t rs = __builtin_amdgcn_make_buffer_rsrc((void*)ws_, (short)0, (int)W_END, 0x00020000);
  auto lds_arrive = [&](int word) INL -> unsigned {
    wait_lds();
    unsigned old = 0;
    if (lane == 0) old = __hip_atomic_fetch_add((LAS unsigned*)(L8 + L_MISC) + word, 1u, __ATOMIC_RELAXED, __HIP_MEMORY_SCOPE_WORKGROUP);
    old = __builtin_amdgcn_readfirstlane(old);
    asm volatile("" ::: "memory");
    return old;
  };
  auto lds_wait_ge = [&](volatile LAS unsigned* p, unsigned need) INL {
    int sp = 0;
    while (*p < need && ++sp < SPIN_LDS) __builtin_amdgcn_s_sleep(1);
    asm volatile("" ::: "memory");
  };
  auto wait_full = [&](int q) INL {
    for (int sp = 0; sp < SPIN_LDS; ++sp) {
      const u32x2 f = FULL2[q & (NSLOT - 1)];
      if (min(f[0], f[1]) > (unsigned)q) break;
      __builtin_amdgcn_s_sleep(1);
    }
    asm volatile("" ::: "memory");
  };

  if (role < NL + NC) {
    const unsigned tag = __builtin_amdgcn_readfirstlane(ep) + 1u;
    {
      float v[8];
#pragma unroll
      for (int i = 0; i < 4; ++i) { v[2 * i] = lo16(xv[i]); v[2 * i + 1] = hi16(xv[i]); }
      float mb, db; ln_stats(v, mb, db);
      float a[8];
#pragma unroll
      for (int k = 0; k < 8; ++k) a[k] = 0.f;
#pragma unroll
      for (int i = 0; i < 4; ++i) {
        const float xn = ln_apply((lane & 2) ? hi16(xr[i]) : lo16(xr[i]), mb, db);
#pragma unroll
        for (int k = 0; k < 4; ++k) { a[2 * k] = fmaf(xn, lo16(wqv[i][k]), a[2 * k]); a[2 * k + 1] = fmaf(xn, hi16(wqv[i][k]), a[2 * k + 1]); }
      }
#pragma unroll
      for (int k = 0; k < 8; ++k) {
        float t = a[k];
        t += dpp<XOR2>(t); t += dpp<ROR4>(t); t += dpp<XOR8>(t);
        a[k] = sum_x16_x32(t);
      }
      LAS float* QP = (LAS float*)(L8 + L_QP);
      if (lane < 2) {
#pragma unroll
        for (int k = 0; k < 8; ++k) QP[cw * 16 + lane * 8 + k] = a[k];
      }
      if (lds_arrive(M_QCNT) == NC - 1) {
        if (lane < 8) {
          const float q0 = (QP[2 * lane] + QP[16 + 2 * lane]) + (QP[32 + 2 * lane] + QP[48 + 2 * lane]);
          const float q1 = (QP[2 * lane + 1] + QP[17 + 2 * lane]) + (QP[33 + 2 * lane] + QP[49 + 2 * lane]);
          gstore2(rs, W_G0 + qchunk * 128 + lane * 16, tag, __float_as_uint(q0), __float_as_uint(q1));
        }
        STAMP(1);
      }
    }
    const unsigned kbase = col * 256 + 16 * (g ^ col);
    bf16x8 qf[4];
    float smax = NEG_BIG;
    auto read_k = [&](int q, u32x4 (&kf)[4][4]) INL {
      wait_full(q);
      const unsigned sb = (unsigned)(q & (NSLOT - 1)) * SLOT_BYTES;
#pragma unroll
      for (int st = 0; st < 4; ++st) {
        const LAS unsigned char* p = L8 + ((sb + kbase) ^ (64u * st));
#pragma unroll
        for (int kg = 0; kg < 4; ++kg) kf[kg][st] = *(const LAS u32x4*)(p + 4096 * kg);
      }
      wait_lds();
      const unsigned done = (unsigned)(q + 1);
      FREE4[q & (NSLOT - 1)] = u32x4{done, done, done, done};
    };
    auto scores = [&](const u32x4 (&kf)[4][4], int ch) INL {
      f32x4 d[4];
#pragma unroll
      for (int kg = 0; kg < 4; ++kg) d[kg] = f32x4{0.f, 0.f, 0.f, 0.f};
#pragma unroll
      for (int st = 0; st < 4; ++st)
#pragma unroll
        for (int kg = 0; kg < 4; ++kg) d[kg] = __builtin_amdgcn_mfma_f32_16x16x32_bf16(__builtin_bit_cast(bf16x8, kf[kg][st]), qf[st], d[kg], 0, 0, 0);
      unsigned c[8];
#pragma unroll
      for (int kg = 0; kg < 4; ++kg) {
        const unsigned a0 = pk2(d[kg][0], d[kg][1]), a1 = pk2(d[kg][2], d[kg][3]);
        const float t0 = lo16(a0) * SCALE, t1 = hi16(a0) * SCALE, t2 = lo16(a1) * SCALE, t3 = hi16(a1) * SCALE;
        c[2 * kg] = pk2(t0, t1); c[2 * kg + 1] = pk2(t2, t3);
        smax = fmaxf(fmaxf(fmaxf(t0, t1), fmaxf(t2, t3)), smax);
      }
      if (col < 4) {
        LAS u32x4* dst = (LAS u32x4*)(L8 + L_SC + ch * 512 + (g * 4 + col) * 32);
        dst[0] = u32x4{c[0], c[1], c[2], c[3]}; dst[1] = u32x4{c[4], c[5], c[6], c[7]};
      }
    };
    {
      u32x4 kfa[4][4], kfb[4][4];
      read_k(cw, kfa);
      read_k(cw + NC, kfb);
      lds_wait_ge(MISC + M_QRDY, 1u);
#pragma unroll
      for (int st = 0; st < 4; ++st) qf[st] = *(const LAS bf16x8*)(L8 + L_Q + (col & 3) * 256 + st * 64 + g * 16);
      if (cw == 0) STAMP(2);
      scores(kfa, cw);
      scores(kfb, cw + NC);
#pragma unroll 1
      for (int q = cw + 2 * NC; q < NCH; q += NC) {
        read_k(q, kfa);
        scores(kfa, q);
      }
    }
    smax = max_x16_x32(smax);
    if (lane < 4) ((LAS float*)(L8 + L_MX))[lane * 4 + cw] = smax;
    lds_arrive(M_MCNT);
    lds_wait_ge(MISC + M_MCNT, NC);
    const f32x4 mx = *(const LAS f32x4*)(L8 + L_MX + (col & 3) * 16);
    const float m = rbf(fmaxf(fmaxf(mx[0], mx[1]), fmaxf(mx[2], mx[3])));
    {
      float lsum = 0.f;
#pragma unroll
      for (int n = 0; n < 2; ++n) {
        LAS u32x4* rec = (LAS u32x4*)(L8 + L_SC + (cw + 4 * (g + 4 * n)) * 512 + col * 32);
#pragma unroll
        for (int t = 0; t < 2; ++t) {
          const u32x4 c = rec[t];
          u32x4 o;
#pragma unroll
          for (int k = 0; k < 4; ++k) {
            const float p0 = __builtin_amdgcn_exp2f((lo16(c[k]) - m) * LOG2E), p1 = __builtin_amdgcn_exp2f((hi16(c[k]) - m) * LOG2E);
            lsum += p0 + p1;
            o[k] = pk2(p0, p1);
          }
          rec[t] = o;
        }
      }
      lsum += dpp<ROR4>(lsum); lsum += dpp<XOR8>(lsum);
      lsum = sum_x16_x32(lsum);
      if (lane < 4) { ((LAS float*)(L8 + L_LS))[lane * 4 + cw] = lsum; if (cw == 0) ((LAS float*)(L8 + L_F))[4 + lane] = m; }
    }
    lds_arrive(M_PCNT);
    lds_wait_ge(MISC + M_PCNT, NC);
    if (cw == 0) STAMP(4);
    const int rh = kvh * 4 + ((j & 7) >> 1), rdq = (j & 1) * 2 + ((j >> 4) & 1), rch = (j >> 3) & 1;
    const unsigned vq = (lane >> 2) & 3, vp = lane & 3;
    const unsigned vbase = (4 * g + vq) * 256 + ((32 * (4 * (g & 1) + vq)) ^ (64u * cw)) + 8 * vp;
    f32x4 acc0 = {0.f, 0.f, 0.f, 0.f}, acc1 = {0.f, 0.f, 0.f, 0.f};
    float fs = 0.f;
    u32x4 wov[4];
#pragma unroll 1
    for (int i = 0; i < NCH; ++i) {
      const int q = NCH + i, s = q & (NSLOT - 1);
      const LAS u32x4* rec = (const LAS u32x4*)(L8 + L_SC + i * 512 + (g * 4 + (col & 3)) * 32);
      const u32x4 p0 = rec[0], p1 = rec[1];
      if (i == NCH - 1) {
        lds_wait_ge(MISC + M_FRDY, 1u);
        fs = ((const LAS float*)(L8 + L_F))[col & 3];
      }
      wait_full(q);
      if (i == NCH - 1) {
#pragma unroll
        for (int k = 0; k < 4; ++k) {
          const int row = (rh * 4 + rdq) * 32 + k * 8 + (lane >> 3);
          wov[k] = __builtin_nontemporal_load((const GAS u32x4*)((const GAS char*)wo_ + (size_t)row * 1024 + rch * 512 + cw * 128 + (lane & 7) * 16));
        }
      }
      const LAS unsigned char* pa = L8 + s * SLOT_BYTES + vbase;
      const LAS unsigned char* pb = L8 + s * SLOT_BYTES + (vbase ^ 32u);
      s16x4 va[4], vb[4];
#pragma unroll
      for (int t = 0; t < 4; ++t) {
        va[t] = __builtin_amdgcn_ds_read_tr16_b64_v4i16((LAS s16x4*)(pa + 4096 * t));
        vb[t] = __builtin_amdgcn_ds_read_tr16_b64_v4i16((LAS s16x4*)(pb + 4096 * t));
      }
      acc0 = __builtin_amdgcn_mfma_f32_16x16x32_bf16(__builtin_bit_cast(bf16x8, __builtin_shufflevector(va[0], va[1], 0, 1, 2, 3, 4, 5, 6, 7)), __builtin_bit_cast(bf16x8, p0), acc0, 0, 0, 0);
      acc1 = __builtin_amdgcn_mfma_f32_16x16x32_bf16(__builtin_bit_cast(bf16x8, __builtin_shufflevector(vb[0], vb[1], 0, 1, 2, 3, 4, 5, 6, 7)), __builtin_bit_cast(bf16x8, p0), acc1, 0, 0, 0);
      acc0 = __builtin_amdgcn_mfma_f32_16x16x32_bf16(__builtin_bit_cast(bf16x8, __builtin_shufflevector(va[2], va[3], 0, 1, 2, 3, 4, 5, 6, 7)), __builtin_bit_cast(bf16x8, p1), acc0, 0, 0, 0);
      acc1 = __builtin_amdgcn_mfma_f32_16x16x32_bf16(__builtin_bit_cast(bf16x8, __builtin_shufflevector(vb[2], vb[3], 0, 1, 2, 3, 4, 5, 6, 7)), __builtin_bit_cast(bf16x8, p1), acc1, 0, 0, 0);
      wait_lds();
      FREE[s * 4 + cw] = q + 1;
    }
    {
      const f32x4 a = (col & 8) ? acc1 : acc0;
      const float v0 = ((col & 4) ? a[2] : a[0]) * fs, v1 = ((col & 4) ? a[3] : a[1]) * fs;
      gstore2(rs, W_G1 + ((kvh * 4 + (col & 3)) * 32 + j) * 1024 + (32 * cw + 2 * (col & 8) + 4 * g + ((col & 4) >> 1)) * 8, tag, __float_as_uint(v0), __float_as_uint(v1));
    }
    if (cw == 0) STAMP(5);
    LAS float* RED = (LAS float*)(L8 + L_RED);
    f32x2 wf[4][4];
#pragma unroll
    for (int i = 0; i < 4; ++i)
#pragma unroll
      for (int k = 0; k < 4; ++k) { wf[i][k] = f32x2{lo16(wov[i][k]), hi16(wov[i][k])}; asm volatile("" : "+v"(wf[i][k])); }
    {
      unsigned off[2]; u32x4 a[2];
#pragma unroll
      for (int k = 0; k < 2; ++k) off[k] = W_G1 + (rh * 32 + cw * 8 + 4 * k + g) * 1024 + rdq * 256 + col * 16;
      gather2<2, 0>(rs, off, tag, a);
      const float o0 = __uint_as_float(a[0][0]) + __uint_as_float(a[1][0]), o1 = __uint_as_float(a[0][2]) + __uint_as_float(a[1][2]);
      const float t = tr16(o0, o1);
      const float r = tr32(t, t);
      if (lane < 32) RED[(2 * col + g) * 4 + cw] = r;
    }
    if (cw == 0) STAMP(6);
    wg_barrier();
    {
      float o[4];
#pragma unroll
      for (int i = 0; i < 4; ++i) { const f32x4 rr = *(const LAS f32x4*)(RED + (i * 8 + (lane >> 3)) * 4); o[i] = (rr[0] + rr[1]) + (rr[2] + rr[3]); }
      const unsigned p01 = pk2(o[0], o[1]), p23 = pk2(o[2], o[3]);
      o[0] = lo16(p01); o[1] = hi16(p01); o[2] = lo16(p23); o[3] = hi16(p23);
      f32x2 a[4];
#pragma unroll
      for (int k = 0; k < 4; ++k) a[k] = (wf[0][k] * o[0] + wf[1][k] * o[1]) + (wf[2][k] * o[2] + wf[3][k] * o[3]);
      const float c0 = tr32(a[0][0], a[2][0]), c1 = tr32(a[0][1], a[2][1]), c2 = tr32(a[1][0], a[3][0]), c3 = tr32(a[1][1], a[3][1]);
      float r0 = tr16(c0, c2), r1 = tr16(c1, c3);
      r0 += dpp<XOR8>(r0); r1 += dpp<XOR8>(r1);
      if (!(lane & 8)) gstore2(rs, W_G2 + ((rh * 4 + rdq) * 512 + rch * 256 + cw * 64 + (lane & 7) * 8 + g * 2) * 8, tag, __float_as_uint(r0), __float_as_uint(r1));
    }
    if (cw == 0) STAMP(7);
    FLUSH();
  } else {
    constexpr int SUBS = 16 / FC, LPP = FC / 2;
    const int bx = b & 7, bi = b >> 3;
    const bool is_final = bi < 4 * SUBS;
    const int fb = (((bi / SUBS) * 8 + bx) * SUBS + (bi % SUBS)) & (512 / FC - 1);
    const unsigned xcol = *(const GAS unsigned*)(x + FC * fb + 2 * (lane & (LPP - 1)));
    const unsigned tag = __builtin_amdgcn_readfirstlane(ep) + 1u;
    {
      { int sp = 0; while (MISC[M_QCNT] < (unsigned)NC && ++sp < SPIN_LDS) __builtin_amdgcn_s_sleep(2); }
      unsigned off[4]; u32x4 a[4];
#pragma unroll
      for (int k = 0; k < 4; ++k) off[k] = W_G0 + kvh * 4096 + k * 1024 + lane * 16;
      __builtin_amdgcn_s_sleep(QPRE);
      gather2<4, 16>(rs, off, tag, a);
#pragma unroll
      for (int k = 0; k < 4; ++k) ((LAS unsigned*)(L8 + L_Q))[k * 64 + lane] = pk2(__uint_as_float(a[k][0]), __uint_as_float(a[k][2]));
      wait_lds();
      MISC[M_QRDY] = 1u;
    }
    float xl0 = lo16(xcol), xl1 = hi16(xcol), gl0 = 0.f, gl1 = 0.f;
    if (is_final) {
      float v[8];
#pragma unroll
      for (int i = 0; i < 4; ++i) { v[2 * i] = lo16(xv[i]); v[2 * i + 1] = hi16(xv[i]); }
      float mb, db; ln_stats(v, mb, db);
#pragma unroll
      for (int i = 0; i < 8; ++i) v[i] = ln_apply(v[i], mb, db);
      float mb2, db2; ln_stats(v, mb2, db2);
      const float hl0 = ln_apply(ln_apply(xl0, mb, db), mb2, db2), hl1 = ln_apply(ln_apply(xl1, mb, db), mb2, db2);
      const GAS u32x2* w1p = (const GAS u32x2*)w1_; const GAS u32x2* b1p = (const GAS u32x2*)b1_; const GAS u32x2* w2p = (const GAS u32x2*)w2_;
      u32x2 wa[7], wb[7], wc[7];
#pragma unroll
      for (int it = 0; it < 7; ++it) { wa[it] = w1p[it * 64 + lane]; wb[it] = b1p[it * 64 + lane]; wc[it] = w2p[it * 64 + lane]; }
      const float b2v = bf2f(b2_[0]);
#pragma unroll 1
      for (int cc = 0; cc < LPP; ++cc) {
        const float h0 = __builtin_bit_cast(float, __builtin_amdgcn_readlane(__builtin_bit_cast(int, hl0), cc));
        const float h1 = __builtin_bit_cast(float, __builtin_amdgcn_readlane(__builtin_bit_cast(int, hl1), cc));
        float y0 = 0.f, y1 = 0.f;
#pragma unroll
        for (int it = 0; it < 7; ++it) {
#pragma unroll
          for (int k = 0; k < 4; ++k) {
            const float w1v = (k & 1) ? hi16(wa[it][k >> 1]) : lo16(wa[it][k >> 1]);
            const float b1v = (k & 1) ? hi16(wb[it][k >> 1]) : lo16(wb[it][k >> 1]);
            const float w2v = (k & 1) ? hi16(wc[it][k >> 1]) : lo16(wc[it][k >> 1]);
            const unsigned t = pk2(h0 * w1v, h1 * w1v);
            const unsigned uu = pk2(lo16(t) + b1v, hi16(t) + b1v);
            const float u0 = lo16(uu), u1 = hi16(uu);
            const float z0 = u0 * fmaf(0.044715f * u0, u0, 1.f), z1 = u1 * fmaf(0.044715f * u1, u1, 1.f);
            constexpr float CZ = -2.f * 0.7978845608028654f * LOG2E;
            const float e0 = __builtin_amdgcn_exp2f(z0 * CZ), e1 = __builtin_amdgcn_exp2f(z1 * CZ);
            const unsigned gg = pk2(u0 * __builtin_amdgcn_rcpf(1.f + e0), u1 * __builtin_amdgcn_rcpf(1.f + e1));
            y0 = fmaf(lo16(gg), w2v, y0); y1 = fmaf(hi16(gg), w2v, y1);
          }
        }
        y0 = rbf(rbf(wave_sum(y0)) + b2v); y1 = rbf(rbf(wave_sum(y1)) + b2v);
        const float g0 = rbf(y0 / (1.f + __builtin_amdgcn_exp2f(-y0 * LOG2E))), g1 = rbf(y1 / (1.f + __builtin_amdgcn_exp2f(-y1 * LOG2E)));
        gl0 = ((lane & (LPP - 1)) == cc) ? g0 : gl0; gl1 = ((lane & (LPP - 1)) == cc) ? g1 : gl1;
      }
    }
    asm volatile("" : "+v"(gl0), "+v"(xl0), "+v"(gl1), "+v"(xl1));
    {
      { int sp = 0; while (MISC[M_PCNT] < (unsigned)NC && ++sp < SPIN_LDS) __builtin_amdgcn_s_sleep(4); }
      asm volatile("" ::: "memory");
      const f32x4 ls = *(const LAS f32x4*)(L8 + L_LS + (lane & 3) * 16);
      const float mown = ((const LAS float*)(L8 + L_F))[4 + (lane & 3)];
      if (lane < 8) gstore2(rs, W_GM + b * 128 + lane * 16, tag, __float_as_uint(mown), __float_as_uint((ls[0] + ls[1]) + (ls[2] + ls[3])));
      unsigned off[2]; u32x4 a[2];
#pragma unroll
      for (int k = 0; k < 2; ++k) off[k] = W_GM + (kvh * 32 + k * 16 + (lane >> 2)) * 128 + (lane & 3) * 16;
      { int sp = 0; while (FULL[(STQ & (NSLOT - 1)) * 2] < (unsigned)(STQ + 1) && ++sp < SPIN_LDS) __builtin_amdgcn_s_sleep(8); }
      gather2<2, 16>(rs, off, tag, a);
      const float m0 = __uint_as_float(a[0][0]), m1 = __uint_as_float(a[1][0]);
      float M = fmaxf(m0, m1);
      M = fmaxf(M, dpp<ROR4>(M)); M = fmaxf(M, dpp<XOR8>(M)); M = max_x16_x32(M);
      float L = fmaf(__builtin_amdgcn_exp2f((m0 - M) * LOG2E), __uint_as_float(a[0][2]), __builtin_amdgcn_exp2f((m1 - M) * LOG2E) * __uint_as_float(a[1][2]));
      L += dpp<ROR4>(L); L += dpp<XOR8>(L); L = sum_x16_x32(L);
      if (lane < 4) ((LAS float*)(L8 + L_F))[lane] = __builtin_amdgcn_exp2f((mown - M) * LOG2E) / L;
      wait_lds();
      MISC[M_FRDY] = 1u;
      if (b == 0) {
        unsigned oe[4]; u32x4 ae[4];
#pragma unroll
        for (int k = 0; k < 4; ++k) oe[k] = W_GM + (k * 64 + lane) * 128;
        gather2<4, 16>(rs, oe, tag, ae);
        if (lane == 0) __hip_atomic_store((GAS unsigned*)(ws + W_EPOCH), tag, __ATOMIC_RELAXED, __HIP_MEMORY_SCOPE_AGENT);
      }
    }
    wg_barrier();
    if (is_final) {
      constexpr int NL2 = FC, PPI = 64 / LPP;
      unsigned off[NL2]; u32x4 a[NL2];
#pragma unroll
      for (int k = 0; k < NL2; ++k) off[k] = W_G2 + ((k * PPI + lane / LPP) * 512 + FC * fb + 2 * (lane & (LPP - 1))) * 8;
      gather2<NL2, 0>(rs, off, tag, a);
      f32x2 r[NL2];
#pragma unroll
      for (int k = 0; k < NL2; ++k) r[k] = f32x2{__uint_as_float(a[k][0]), __uint_as_float(a[k][2])};
#pragma unroll
      for (int w = 1; w < NL2; w *= 2)
#pragma unroll
        for (int k = 0; k < NL2; k += 2 * w) r[k] += r[k + w];
      float sv = tr32(r[0][0], r[0][1]);
      sv = tr16(sv, sv);
      sv += dpp<XOR8>(sv);
      if (LPP <= 4) sv += dpp<ROR4>(sv);
      if (LPP <= 2) sv += dpp<XOR2>(sv);
      if (LPP <= 1) sv += dpp<XOR1>(sv);
      const float res = (lane < 32 ? xl0 : xl1) + rbf(rbf(sv) * (lane < 32 ? gl0 : gl1));
      auto sw = __builtin_amdgcn_permlane32_swap(__float_as_uint(res), __float_as_uint(res), false, false);
      if (lane < LPP) out_[LPP * fb + lane] = pk2(__uint_as_float(sw[0]), __uint_as_float(sw[1]));
    }
    STAMP(8);
    FLUSH();
  }
  __builtin_amdgcn_s_waitcnt(0x0F70);
}

extern "C" void kernel_launch(void* const* d_in, const int* in_sizes, int n_in, void* d_out, int out_size, void* d_ws, size_t ws_size,
                              hipStream_t stream) {
  fused_decode<<<256, THREADS, 0, stream>>>((const char*)d_in[1], (char*)d_ws, (const u16*)d_in[0], (const char*)d_in[3],
                                            (const char*)d_in[2], (const char*)d_in[4], (unsigned*)d_out, (const u16*)d_in[6],
                                            (const u16*)d_in[7], (const u16*)d_in[8], (const u16*)d_in[9]);
}
```

```cpp
#include <hip/hip_runtime.h>
#include <hip/hip_bf16.h>
#include <stdint.h>

#define LAS __attribute__((address_space(3)))
#define GAS __attribute__((address_space(1)))
#define INL __attribute__((always_inline))
typedef unsigned short u16;
using u32x4 = __attribute__((ext_vector_type(4))) unsigned;
using u32x2 = __attribute__((ext_vector_type(2))) unsigned;
using f32x4 = __attribute__((ext_vector_type(4))) float;
using f32x2 = __attribute__((ext_vector_type(2))) float;
using bf16x2 = __attribute__((ext_vector_type(2))) __bf16;
using bf16x8 = __attribute__((ext_vector_type(8))) __bf16;
using s16x4 = __attribute__((ext_vector_type(4))) short;
typedef __amdgpu_buffer_rsrc_t rsrc_t;

constexpr int NL = 2, NC = 4;
constexpr int THREADS = (NL + NC + 1) * 64;
constexpr int NSLOT = 8;
constexpr int SLOT_BYTES = 16384;
constexpr int NCH = 32;
constexpr int RING = NSLOT * SLOT_BYTES;
constexpr int NPRE = 2;
constexpr int WA = 10, WB = 11, WC = 12, WD = 13, WE = 15, WF = 18;
constexpr int T0 = 0, T1 = 40, T2 = 70, T3 = 100, T4 = 130;
constexpr int AT0 = 150, ATAU = 845;
constexpr int TARGET = 80;
constexpr int QPRE = 56, PRE2 = 36;
constexpr int STQ = 38;
constexpr int FC = 2;
constexpr int L_SC = RING;
constexpr int L_FREE = L_SC + 16384;
constexpr int L_FULL = L_FREE + 128;
constexpr int L_MISC = L_FULL + 64;
constexpr int L_QP = L_MISC + 64;
constexpr int L_MX = L_QP + 256;
constexpr int L_LS = L_MX + 64;
constexpr int L_F = L_LS + 64;
constexpr int L_RED = L_F + 64;
constexpr int L_Q = L_RED + 512;
constexpr int LDS_BYTES = L_Q + 1024;
static_assert(LDS_BYTES <= 163840, "LDS");
constexpr int M_QCNT = 0, M_QRDY = 1, M_MCNT = 2, M_PCNT = 3, M_FRDY = 4, M_TH = 5, M_BASE = 6;
constexpr unsigned W_EPOCH = 0;
constexpr unsigned W_G0 = 4096;
constexpr unsigned W_GM = W_G0 + 32768;
constexpr unsigned W_G2 = W_GM + 32768;
constexpr unsigned W_G1 = W_G2 + 524288;
constexpr unsigned W_END = W_G1 + 1048576;

constexpr int SPIN_LDS = 1 << 20;
constexpr int SPIN_GLB = 1 << 17;
constexpr float LOG2E = 1.4426950408889634f;
constexpr float SCALE = 0.08838834764831845f;
constexpr float NEG_BIG = -3.0e38f;

__device__ __forceinline__ float bf2f(unsigned b) { return __uint_as_float(b << 16); }
__device__ __forceinline__ unsigned pk2(float lo, float hi) {
  f32x2 p = {lo, hi};
  return __builtin_bit_cast(unsigned, __builtin_convertvector(p, bf16x2));
}
__device__ __forceinline__ float rbf(float f) { return __uint_as_float(pk2(f, f) << 16); }
__device__ __forceinline__ float lo16(unsigned u) { return __uint_as_float(u << 16); }
__device__ __forceinline__ float hi16(unsigned u) { return __uint_as_float(u & 0xffff0000u); }

template <int CTRL> __device__ __forceinline__ float dpp(float x) {
  return __builtin_bit_cast(float, __builtin_amdgcn_mov_dpp(__builtin_bit_cast(int, x), CTRL, 0xf, 0xf, true));
}
constexpr int XOR1 = 0xB1, XOR2 = 0x4E, XOR7 = 0x141, ROR4 = 0x124, XOR8 = 0x128;
__device__ __forceinline__ float tr16(float a, float b) {
  auto s = __builtin_amdgcn_permlane16_swap(__float_as_uint(a), __float_as_uint(b), false, false);
  return __uint_as_float(s[0]) + __uint_as_float(s[1]);
}
__device__ __forceinline__ float tr32(float a, float b) {
  auto s = __builtin_amdgcn_permlane32_swap(__float_as_uint(a), __float_as_uint(b), false, false);
  return __uint_as_float(s[0]) + __uint_as_float(s[1]);
}
__device__ __forceinline__ float sum_x16_x32(float x) { x = tr16(x, x); return tr32(x, x); }
__device__ __forceinline__ float max_x16_x32(float x) {
  auto s = __builtin_amdgcn_permlane16_swap(__float_as_uint(x), __float_as_uint(x), false, false);
  x = fmaxf(__uint_as_float(s[0]), __uint_as_float(s[1]));
  auto t = __builtin_amdgcn_permlane32_swap(__float_as_uint(x), __float_as_uint(x), false, false);
  return fmaxf(__uint_as_float(t[0]), __uint_as_float(t[1]));
}
__device__ __forceinline__ float wave_sum(float x) {
  x += dpp<XOR1>(x); x += dpp<XOR2>(x); x += dpp<XOR7>(x); x += dpp<XOR8>(x);
  return sum_x16_x32(x);
}

template <int N> struct int_c { static constexpr int value = N; };
template <int N> __device__ __forceinline__ void wait_vm() { asm volatile("s_waitcnt vmcnt(%0)" ::"i"(N) : "memory"); }
__device__ __forceinline__ void wait_lds() { asm volatile("s_waitcnt lgkmcnt(0)" ::: "memory"); }
__device__ __forceinline__ void wg_barrier() { asm volatile("s_waitcnt lgkmcnt(0)\n\ts_barrier" ::: "memory"); }
#define GLDS(src, dst) __builtin_amdgcn_global_load_lds((const GAS unsigned*)(src), (LAS unsigned*)(dst), 16, 0, 2)

__device__ __forceinline__ void gstore2(rsrc_t rs, unsigned off, unsigned tag, unsigned v0, unsigned v1) {
  const u32x4 v = {v0, tag, v1, tag};
  __builtin_amdgcn_raw_buffer_store_b128(v, rs, off, 0, 16);
}
template <int N, int SLEEP>
__device__ __forceinline__ void gather2(rsrc_t rs, const unsigned (&off)[N], unsigned tag, u32x4 (&a)[N]) {
#pragma unroll 1
  for (int sp = 0; sp < SPIN_GLB; ++sp) {
#pragma unroll
    for (int k = 0; k < N; ++k) a[k] = __builtin_amdgcn_raw_buffer_load_b128(rs, off[k], 0, 16);
    unsigned miss = 0u;
#pragma unroll
    for (int k = 0; k < N; ++k) miss |= (a[k][1] ^ tag) | (a[k][3] ^ tag);
    asm volatile("" ::: "memory");
    if (__all(miss == 0u)) break;
    if (SLEEP) __builtin_amdgcn_s_sleep(SLEEP);
  }
}

__device__ __forceinline__ void ln_stats(const float (&v)[8], float& mean_b, float& den_b) {
  float s = 0.f;
#pragma unroll
  for (int i = 0; i < 8; ++i) s += v[i];
  s = wave_sum(s);
  const float mean = s * (1.f / 512.f);
  float q = 0.f;
#pragma unroll
  for (int i = 0; i < 8; ++i) { const float d = v[i] - mean; q = fmaf(d, d, q); }
  q = wave_sum(q);
  const float var = rbf(q * (1.f / 512.f));
  mean_b = rbf(mean);
  den_b = rbf(sqrtf(rbf(var + 1e-5f)));
}
__device__ __forceinline__ float ln_apply(float x, float mean_b, float den_b) { return rbf(rbf(x - mean_b) / den_b); }

__device__ __forceinline__ int chunk_of(int i, int j) { return (i >> 3) * 256 + j * 8 + ((i + j) & 7); }

__global__ void __launch_bounds__(THREADS, 2)
fused_decode(const char* __restrict__ kc_, char* ws_, const u16* __restrict__ x_, const char* __restrict__ wq_,
             const char* __restrict__ vc_, const char* __restrict__ wo_, unsigned* __restrict__ out_, const u16* __restrict__ w1_,
             const u16* __restrict__ b1_, const u16* __restrict__ w2_, const u16* __restrict__ b2_) {
  __shared__ __attribute__((aligned(16))) unsigned char lds[LDS_BYTES];
  LAS unsigned char* L8 = (LAS unsigned char*)lds;
  volatile LAS unsigned* FULL = (volatile LAS unsigned*)(L8 + L_FULL);
  volatile LAS u32x2* FULL2 = (volatile LAS u32x2*)(L8 + L_FULL);
  volatile LAS u32x4* FREE4 = (volatile LAS u32x4*)(L8 + L_FREE);
  volatile LAS unsigned* FREE = (volatile LAS unsigned*)(L8 + L_FREE);
  volatile LAS unsigned* MISC = (volatile LAS unsigned*)(L8 + L_MISC);
  const GAS u16* x = (const GAS u16*)x_;
  GAS char* ws = (GAS char*)ws_;
  const int tid = threadIdx.x, lane = tid & 63, wave = __builtin_amdgcn_readfirstlane(tid >> 6);
  const int b = blockIdx.x, kvh = b >> 5, j = b & 31;
  const unsigned tentry = (unsigned)__builtin_amdgcn_s_memrealtime();
  if (tid < 64) ((LAS unsigned*)(L8 + L_FREE))[lane] = 0u;
  asm volatile("" :: "s"(kc_), "s"(ws_), "s"(x_), "s"(wq_));
  wg_barrier();
  const GAS char* kb = (const GAS char*)kc_ + (size_t)kvh * (65536 * 256) + wave * 8192;
  const unsigned r4 = lane >> 4, c16 = lane & 15;
  const unsigned koff = r4 * 256 + 16 * (c16 ^ r4);
  if (wave < NL) {
#pragma unroll 1
    for (int u = 0; u < 2 * NPRE; ++u) {
      const GAS char* src = kb + (size_t)chunk_of(u >> 1, j) * SLOT_BYTES + (u & 1) * 4096;
      LAS unsigned char* dst = L8 + (u >> 1) * SLOT_BYTES + wave * 8192 + (u & 1) * 4096;
      GLDS(src + koff, dst);
      GLDS(src + 1024 + (koff ^ 64u), dst + 1024);
      GLDS(src + 2048 + (koff ^ 128u), dst + 2048);
      GLDS(src + 3072 + (koff ^ 192u), dst + 3072);
    }
  }
  const int rh = kvh * 4 + ((j & 7) >> 1), rdq = (j & 1) * 2 + ((j >> 4) & 1), rch = (j >> 3) & 1;
  int role = wave;
  asm volatile("" : "+s"(role));
  if (__builtin_expect(role < NL, 0)) {
    int pub = 0;
    unsigned tbase = tentry + AT0;
    auto publish = [&]() INL { FULL[(pub & (NSLOT - 1)) * 2 + wave] = (unsigned)(pub + 1); ++pub; };
    auto run = [&](int q0, int q1, const GAS char* base, unsigned o0, unsigned o1, unsigned o2, unsigned o3) INL {
#pragma unroll 1
      for (int q = q0; q < q1; ++q) {
        const int s = q & (NSLOT - 1);
        if (q >= NSLOT) {
          const unsigned need = (unsigned)(q - NSLOT + 1);
          u32x4 f = FREE4[s];
          if (min(min(f[0], f[1]), min(f[2], f[3])) < need) {
            if (pub < q) { wait_vm<0>(); while (pub < q) publish(); }
            for (int sp = 0; sp < SPIN_LDS; ++sp) {
              f = FREE4[s];
              if (min(min(f[0], f[1]), min(f[2], f[3])) >= need) break;
              __builtin_amdgcn_s_sleep(1);
            }
          }
        }
        const GAS char* src = base + (size_t)chunk_of(q & (NCH - 1), j) * SLOT_BYTES;
        LAS unsigned char* dst = L8 + s * SLOT_BYTES + wave * 8192;
        auto fills = [&](auto win) INL {
          constexpr int W = decltype(win)::value;
#pragma unroll
          for (int i = 0; i < 8; ++i) {
            wait_vm<W - 1>();
            if (((i - (W - 1)) & 7) == 0) { while (((8 * q + i - (W - 1)) >> 3) > pub) publish(); }
            GLDS(src + i * 1024 + ((i & 3) == 0 ? o0 : (i & 3) == 1 ? o1 : (i & 3) == 2 ? o2 : o3), dst + i * 1024);
          }
        };
        const unsigned now = (unsigned)__builtin_amdgcn_s_memrealtime();
        if (q == NCH && wave == 0) MISC[M_TH] = now | 1u;
        if (q > STQ) { const unsigned nb = __builtin_amdgcn_readfirstlane(MISC[M_BASE]); if (nb != 0u) tbase = nb; }
        const int lag = (int)(now - tbase) * 16 - ATAU * q;
        if (lag <= T2 * 16) {
          if (lag <= T0 * 16) fills(int_c<WA>{}); else if (lag <= T1 * 16) fills(int_c<WB>{}); else fills(int_c<WC>{});
        } else {
          if (lag <= T3 * 16) fills(int_c<WD>{}); else if (lag <= T4 * 16) fills(int_c<WE>{}); else fills(int_c<WF>{});
        }
      }
    };
    run(NPRE, NCH, kb, koff, koff ^ 64u, koff ^ 128u, koff ^ 192u);
    const GAS char* vb = (const GAS char*)vc_ + (size_t)kvh * (65536 * 256) + wave * 8192;
    const unsigned voff = r4 * 256 + 16 * (c16 ^ (2 * r4));
    run(NCH, 2 * NCH, vb, voff, voff ^ 128u, voff, voff ^ 128u);
    if (pub <= 2 * NCH - 3) { wait_vm<16>(); while (pub < 2 * NCH - 2) publish(); }
    if (pub == 2 * NCH - 2) { wait_vm<8>(); publish(); }
    wait_vm<0>();
    while (pub < 2 * NCH) publish();
    wg_barrier();
    return;
  }

  const int cw = (wave - NL) & 3;
  const int qchunk = kvh * 32 + (j & 7) * 4 + (j >> 3);
  u32x4 wqv[4]; unsigned xr[4];
  const u32x4 xv = *(const GAS u32x4*)(x + lane * 8);
  const unsigned ep = __hip_atomic_load((const GAS unsigned*)(ws + W_EPOCH), __ATOMIC_RELAXED, __HIP_MEMORY_SCOPE_AGENT);
#pragma unroll
  for (int i = 0; i < 4; ++i) xr[i] = *(const GAS unsigned*)(x + ((cw * 128 + i * 32 + (lane >> 1)) & ~1));
  if (wave < NL + NC) {
#pragma unroll
    for (int i = 0; i < 4; ++i) {
      const int row = cw * 128 + i * 32 + (lane >> 1);
      wqv[i] = __builtin_nontemporal_load((const GAS u32x4*)((const GAS char*)wq_ + (size_t)row * 8192 + qchunk * 32 + (lane & 1) * 16));
    }
  }
  asm volatile("" : "+s"(role) :: "memory");
  const unsigned col = lane & 15, g = lane >> 4;
  const rsrc_t rs = __builtin_amdgcn_make_buffer_rsrc((void*)ws_, (short)0, (int)W_END, 0x00020000);
  auto lds_arrive = [&](int word) INL -> unsigned {
    wait_lds();
    unsigned old = 0;
    if (lane == 0) old = __hip_atomic_fetch_add((LAS unsigned*)(L8 + L_MISC) + word, 1u, __ATOMIC_RELAXED, __HIP_MEMORY_SCOPE_WORKGROUP);
    old = __builtin_amdgcn_readfirstlane(old);
    asm volatile("" ::: "memory");
    return old;
  };
  auto lds_wait_ge = [&](volatile LAS unsigned* p, unsigned need) INL {
    int sp = 0;
    while (*p < need && ++sp < SPIN_LDS) __builtin_amdgcn_s_sleep(1);
    asm volatile("" ::: "memory");
  };
  auto wait_full = [&](int q) INL {
    for (int sp = 0; sp < SPIN_LDS; ++sp) {
      const u32x2 f = FULL2[q & (NSLOT - 1)];
      if (min(f[0], f[1]) > (unsigned)q) break;
      __builtin_amdgcn_s_sleep(1);
    }
    asm volatile("" ::: "memory");
  };

  if (role < NL + NC) {
    const unsigned tag = __builtin_amdgcn_readfirstlane(ep) + 1u;
    {
      float v[8];
#pragma unroll
      for (int i = 0; i < 4; ++i) { v[2 * i] = lo16(xv[i]); v[2 * i + 1] = hi16(xv[i]); }
      float mb, db; ln_stats(v, mb, db);
      float a[8];
#pragma unroll
      for (int k = 0; k < 8; ++k) a[k] = 0.f;
#pragma unroll
      for (int i = 0; i < 4; ++i) {
        const float xn = ln_apply((lane & 2) ? hi16(xr[i]) : lo16(xr[i]), mb, db);
#pragma unroll
        for (int k = 0; k < 4; ++k) { a[2 * k] = fmaf(xn, lo16(wqv[i][k]), a[2 * k]); a[2 * k + 1] = fmaf(xn, hi16(wqv[i][k]), a[2 * k + 1]); }
      }
#pragma unroll
      for (int k = 0; k < 8; ++k) {
        float t = a[k];
        t += dpp<XOR2>(t); t += dpp<ROR4>(t); t += dpp<XOR8>(t);
        a[k] = sum_x16_x32(t);
      }
      LAS float* QP = (LAS float*)(L8 + L_QP);
      if (lane < 2) {
#pragma unroll
        for (int k = 0; k < 8; ++k) QP[cw * 16 + lane * 8 + k] = a[k];
      }
      if (lds_arrive(M_QCNT) == NC - 1) {
        if (lane < 8) {
          const float q0 = (QP[2 * lane] + QP[16 + 2 * lane]) + (QP[32 + 2 * lane] + QP[48 + 2 * lane]);
          const float q1 = (QP[2 * lane + 1] + QP[17 + 2 * lane]) + (QP[33 + 2 * lane] + QP[49 + 2 * lane]);
          gstore2(rs, W_G0 + qchunk * 128 + lane * 16, tag, __float_as_uint(q0), __float_as_uint(q1));
        }
      }
    }
    const unsigned kbase = col * 256 + 16 * (g ^ col);
    bf16x8 qf[4];
    float smax = NEG_BIG;
    auto read_k = [&](int q, u32x4 (&kf)[4][4]) INL {
      wait_full(q);
      const unsigned sb = (unsigned)(q & (NSLOT - 1)) * SLOT_BYTES;
#pragma unroll
      for (int st = 0; st < 4; ++st) {
        const LAS unsigned char* p = L8 + ((sb + kbase) ^ (64u * st));
#pragma unroll
        for (int kg = 0; kg < 4; ++kg) kf[kg][st] = *(const LAS u32x4*)(p + 4096 * kg);
      }
      wait_lds();
      const unsigned done = (unsigned)(q + 1);
      FREE4[q & (NSLOT - 1)] = u32x4{done, done, done, done};
    };
    auto scores = [&](const u32x4 (&kf)[4][4], int ch) INL {
      f32x4 d[4];
#pragma unroll
      for (int kg = 0; kg < 4; ++kg) d[kg] = f32x4{0.f, 0.f, 0.f, 0.f};
#pragma unroll
      for (int st = 0; st < 4; ++st)
#pragma unroll
        for (int kg = 0; kg < 4; ++kg) d[kg] = __builtin_amdgcn_mfma_f32_16x16x32_bf16(__builtin_bit_cast(bf16x8, kf[kg][st]), qf[st], d[kg], 0, 0, 0);
      unsigned c[8];
#pragma unroll
      for (int kg = 0; kg < 4; ++kg) {
        const unsigned a0 = pk2(d[kg][0], d[kg][1]), a1 = pk2(d[kg][2], d[kg][3]);
        const float t0 = lo16(a0) * SCALE, t1 = hi16(a0) * SCALE, t2 = lo16(a1) * SCALE, t3 = hi16(a1) * SCALE;
        c[2 * kg] = pk2(t0, t1); c[2 * kg + 1] = pk2(t2, t3);
        smax = fmaxf(fmaxf(fmaxf(t0, t1), fmaxf(t2, t3)), smax);
      }
      if (col < 4) {
        LAS u32x4* dst = (LAS u32x4*)(L8 + L_SC + ch * 512 + (g * 4 + col) * 32);
        dst[0] = u32x4{c[0], c[1], c[2], c[3]}; dst[1] = u32x4{c[4], c[5], c[6], c[7]};
      }
    };
    {
      u32x4 kfa[4][4], kfb[4][4];
      read_k(cw, kfa);
      read_k(cw + NC, kfb);
      lds_wait_ge(MISC + M_QRDY, 1u);
#pragma unroll
      for (int st = 0; st < 4; ++st) qf[st] = *(const LAS bf16x8*)(L8 + L_Q + (col & 3) * 256 + st * 64 + g * 16);
      scores(kfa, cw);
      scores(kfb, cw + NC);
#pragma unroll 1
      for (int q = cw + 2 * NC; q < NCH; q += NC) {
        read_k(q, kfa);
        scores(kfa, q);
      }
    }
    smax = max_x16_x32(smax);
    if (lane < 4) ((LAS float*)(L8 + L_MX))[lane * 4 + cw] = smax;
    lds_arrive(M_MCNT);
    lds_wait_ge(MISC + M_MCNT, NC);
    const f32x4 mx = *(const LAS f32x4*)(L8 + L_MX + (col & 3) * 16);
    const float m = rbf(fmaxf(fmaxf(mx[0], mx[1]), fmaxf(mx[2], mx[3])));
    {
      float lsum = 0.f;
#pragma unroll
      for (int n = 0; n < 2; ++n) {
        LAS u32x4* rec = (LAS u32x4*)(L8 + L_SC + (cw + 4 * (g + 4 * n)) * 512 + col * 32);
#pragma unroll
        for (int t = 0; t < 2; ++t) {
          const u32x4 c = rec[t];
          u32x4 o;
#pragma unroll
          for (int k = 0; k < 4; ++k) {
            const float p0 = __builtin_amdgcn_exp2f((lo16(c[k]) - m) * LOG2E), p1 = __builtin_amdgcn_exp2f((hi16(c[k]) - m) * LOG2E);
            lsum += p0 + p1;
            o[k] = pk2(p0, p1);
          }
          rec[t] = o;
        }
      }
      lsum += dpp<ROR4>(lsum); lsum += dpp<XOR8>(lsum);
      lsum = sum_x16_x32(lsum);
      if (lane < 4) { ((LAS float*)(L8 + L_LS))[lane * 4 + cw] = lsum; if (cw == 0) ((LAS float*)(L8 + L_F))[4 + lane] = m; }
    }
    lds_arrive(M_PCNT);
    lds_wait_ge(MISC + M_PCNT, NC);
    const unsigned vq = (lane >> 2) & 3, vp = lane & 3;
    const unsigned vbase = (4 * g + vq) * 256 + ((32 * (4 * (g & 1) + vq)) ^ (64u * cw)) + 8 * vp;
    f32x4 acc0 = {0.f, 0.f, 0.f, 0.f}, acc1 = {0.f, 0.f, 0.f, 0.f};
    float fs = 0.f;
    u32x4 wov[4];
#pragma unroll 1
    for (int i = 0; i < NCH; ++i) {
      const int q = NCH + i, s = q & (NSLOT - 1);
      const LAS u32x4* rec = (const LAS u32x4*)(L8 + L_SC + i * 512 + (g * 4 + (col & 3)) * 32);
      const u32x4 p0 = rec[0], p1 = rec[1];
      if (i == NCH - 1) {
        lds_wait_ge(MISC + M_FRDY, 1u);
        fs = ((const LAS float*)(L8 + L_F))[col & 3];
      }
      wait_full(q);
      if (i == NCH - 1) {
#pragma unroll
        for (int k = 0; k < 4; ++k) {
          const int row = (rh * 4 + rdq) * 32 + k * 8 + (lane >> 3);
          wov[k] = __builtin_nontemporal_load((const GAS u32x4*)((const GAS char*)wo_ + (size_t)row * 1024 + rch * 512 + cw * 128 + (lane & 7) * 16));
        }
      }
      const LAS unsigned char* pa = L8 + s * SLOT_BYTES + vbase;
      const LAS unsigned char* pb = L8 + s * SLOT_BYTES + (vbase ^ 32u);
      s16x4 va[4], vb[4];
#pragma unroll
      for (int t = 0; t < 4; ++t) {
        va[t] = __builtin_amdgcn_ds_read_tr16_b64_v4i16((LAS s16x4*)(pa + 4096 * t));
        vb[t] = __builtin_amdgcn_ds_read_tr16_b64_v4i16((LAS s16x4*)(pb + 4096 * t));
      }
      acc0 = __builtin_amdgcn_mfma_f32_16x16x32_bf16(__builtin_bit_cast(bf16x8, __builtin_shufflevector(va[0], va[1], 0, 1, 2, 3, 4, 5, 6, 7)), __builtin_bit_cast(bf16x8, p0), acc0, 0, 0, 0);
      acc1 = __builtin_amdgcn_mfma_f32_16x16x32_bf16(__builtin_bit_cast(bf16x8, __builtin_shufflevector(vb[0], vb[1], 0, 1, 2, 3, 4, 5, 6, 7)), __builtin_bit_cast(bf16x8, p0), acc1, 0, 0, 0);
      acc0 = __builtin_amdgcn_mfma_f32_16x16x32_bf16(__builtin_bit_cast(bf16x8, __builtin_shufflevector(va[2], va[3], 0, 1, 2, 3, 4, 5, 6, 7)), __builtin_bit_cast(bf16x8, p1), acc0, 0, 0, 0);
      acc1 = __builtin_amdgcn_mfma_f32_16x16x32_bf16(__builtin_bit_cast(bf16x8, __builtin_shufflevector(vb[2], vb[3], 0, 1, 2, 3, 4, 5, 6, 7)), __builtin_bit_cast(bf16x8, p1), acc1, 0, 0, 0);
      wait_lds();
      FREE[s * 4 + cw] = q + 1;
    }
    {
      const f32x4 a = (col & 8) ? acc1 : acc0;
      const float v0 = ((col & 4) ? a[2] : a[0]) * fs, v1 = ((col & 4) ? a[3] : a[1]) * fs;
      gstore2(rs, W_G1 + ((kvh * 4 + (col & 3)) * 32 + j) * 1024 + (32 * cw + 2 * (col & 8) + 4 * g + ((col & 4) >> 1)) * 8, tag, __float_as_uint(v0), __float_as_uint(v1));
    }
    LAS float* RED = (LAS float*)(L8 + L_RED);
    f32x2 wf[4][4];
#pragma unroll
    for (int i = 0; i < 4; ++i)
#pragma unroll
      for (int k = 0; k < 4; ++k) { wf[i][k] = f32x2{lo16(wov[i][k]), hi16(wov[i][k])}; asm volatile("" : "+v"(wf[i][k])); }
    {
      unsigned off[2]; u32x4 a[2];
#pragma unroll
      for (int k = 0; k < 2; ++k) off[k] = W_G1 + (rh * 32 + cw * 8 + 4 * k + g) * 1024 + rdq * 256 + col * 16;
      gather2<2, 0>(rs, off, tag, a);
      const float o0 = __uint_as_float(a[0][0]) + __uint_as_float(a[1][0]), o1 = __uint_as_float(a[0][2]) + __uint_as_float(a[1][2]);
      const float t = tr16(o0, o1);
      const float r = tr32(t, t);
      if (lane < 32) RED[(2 * col + g) * 4 + cw] = r;
    }
    wg_barrier();
    {
      float o[4];
#pragma unroll
      for (int i = 0; i < 4; ++i) { const f32x4 rr = *(const LAS f32x4*)(RED + (i * 8 + (lane >> 3)) * 4); o[i] = (rr[0] + rr[1]) + (rr[2] + rr[3]); }
      const unsigned p01 = pk2(o[0], o[1]), p23 = pk2(o[2], o[3]);
      o[0] = lo16(p01); o[1] = hi16(p01); o[2] = lo16(p23); o[3] = hi16(p23);
      f32x2 a[4];
#pragma unroll
      for (int k = 0; k < 4; ++k) a[k] = (wf[0][k] * o[0] + wf[1][k] * o[1]) + (wf[2][k] * o[2] + wf[3][k] * o[3]);
      const float c0 = tr32(a[0][0], a[2][0]), c1 = tr32(a[0][1], a[2][1]), c2 = tr32(a[1][0], a[3][0]), c3 = tr32(a[1][1], a[3][1]);
      float r0 = tr16(c0, c2), r1 = tr16(c1, c3);
      r0 += dpp<XOR8>(r0); r1 += dpp<XOR8>(r1);
      if (!(lane & 8)) gstore2(rs, W_G2 + ((rh * 4 + rdq) * 512 + rch * 256 + cw * 64 + (lane & 7) * 8 + g * 2) * 8, tag, __float_as_uint(r0), __float_as_uint(r1));
    }
  } else {
    constexpr int SUBS = 16 / FC, LPP = FC / 2;
    const int bx = b & 7, bi = b >> 3;
    const bool is_final = bi < 4 * SUBS;
    const int fb = (((bi / SUBS) * 8 + bx) * SUBS + (bi % SUBS)) & (512 / FC - 1);
    const unsigned xcol = *(const GAS unsigned*)(x + FC * fb + 2 * (lane & (LPP - 1)));
    const unsigned tag = __builtin_amdgcn_readfirstlane(ep) + 1u;
    {
      { int sp = 0; while (MISC[M_QCNT] < (unsigned)NC && ++sp < SPIN_LDS) __builtin_amdgcn_s_sleep(2); }
      unsigned off[4]; u32x4 a[4];
#pragma unroll
      for (int k = 0; k < 4; ++k) off[k] = W_G0 + kvh * 4096 + k * 1024 + lane * 16;
      __builtin_amdgcn_s_sleep(QPRE);
      gather2<4, 16>(rs, off, tag, a);
#pragma unroll
      for (int k = 0; k < 4; ++k) ((LAS unsigned*)(L8 + L_Q))[k * 64 + lane] = pk2(__uint_as_float(a[k][0]), __uint_as_float(a[k][2]));
      wait_lds();
      MISC[M_QRDY] = 1u;
    }
    float xl0 = lo16(xcol), xl1 = hi16(xcol), gl0 = 0.f, gl1 = 0.f;
    if (is_final) {
      float v[8];
#pragma unroll
      for (int i = 0; i < 4; ++i) { v[2 * i] = lo16(xv[i]); v[2 * i + 1] = hi16(xv[i]); }
      float mb, db; ln_stats(v, mb, db);
#pragma unroll
      for (int i = 0; i < 8; ++i) v[i] = ln_apply(v[i], mb, db);
      float mb2, db2; ln_stats(v, mb2, db2);
      const float hl0 = ln_apply(ln_apply(xl0, mb, db), mb2, db2), hl1 = ln_apply(ln_apply(xl1, mb, db), mb2, db2);
      const GAS u32x2* w1p = (const GAS u32x2*)w1_; const GAS u32x2* b1p = (const GAS u32x2*)b1_; const GAS u32x2* w2p = (const GAS u32x2*)w2_;
      u32x2 wa[7], wb[7], wc[7];
#pragma unroll
      for (int it = 0; it < 7; ++it) { wa[it] = w1p[it * 64 + lane]; wb[it] = b1p[it * 64 + lane]; wc[it] = w2p[it * 64 + lane]; }
      const float b2v = bf2f(b2_[0]);
#pragma unroll 1
      for (int cc = 0; cc < LPP; ++cc) {
        const float h0 = __builtin_bit_cast(float, __builtin_amdgcn_readlane(__builtin_bit_cast(int, hl0), cc));
        const float h1 = __builtin_bit_cast(float, __builtin_amdgcn_readlane(__builtin_bit_cast(int, hl1), cc));
        float y0 = 0.f, y1 = 0.f;
#pragma unroll
        for (int it = 0; it < 7; ++it) {
#pragma unroll
          for (int k = 0; k < 4; ++k) {
            const float w1v = (k & 1) ? hi16(wa[it][k >> 1]) : lo16(wa[it][k >> 1]);
            const float b1v = (k & 1) ? hi16(wb[it][k >> 1]) : lo16(wb[it][k >> 1]);
            const float w2v = (k & 1) ? hi16(wc[it][k >> 1]) : lo16(wc[it][k >> 1]);
            const unsigned t = pk2(h0 * w1v, h1 * w1v);
            const unsigned uu = pk2(lo16(t) + b1v, hi16(t) + b1v);
            const float u0 = lo16(uu), u1 = hi16(uu);
            const float z0 = u0 * fmaf(0.044715f * u0, u0, 1.f), z1 = u1 * fmaf(0.044715f * u1, u1, 1.f);
            constexpr float CZ = -2.f * 0.7978845608028654f * LOG2E;
            const float e0 = __builtin_amdgcn_exp2f(z0 * CZ), e1 = __builtin_amdgcn_exp2f(z1 * CZ);
            const unsigned gg = pk2(u0 * __builtin_amdgcn_rcpf(1.f + e0), u1 * __builtin_amdgcn_rcpf(1.f + e1));
            y0 = fmaf(lo16(gg), w2v, y0); y1 = fmaf(hi16(gg), w2v, y1);
          }
        }
        y0 = rbf(rbf(wave_sum(y0)) + b2v); y1 = rbf(rbf(wave_sum(y1)) + b2v);
        const float g0 = rbf(y0 / (1.f + __builtin_amdgcn_exp2f(-y0 * LOG2E))), g1 = rbf(y1 / (1.f + __builtin_amdgcn_exp2f(-y1 * LOG2E)));
        gl0 = ((lane & (LPP - 1)) == cc) ? g0 : gl0; gl1 = ((lane & (LPP - 1)) == cc) ? g1 : gl1;
      }
    }
    asm volatile("" : "+v"(gl0), "+v"(xl0), "+v"(gl1), "+v"(xl1));
    {
      { int sp = 0; while (MISC[M_PCNT] < (unsigned)NC && ++sp < SPIN_LDS) __builtin_amdgcn_s_sleep(4); }
      asm volatile("" ::: "memory");
      const f32x4 ls = *(const LAS f32x4*)(L8 + L_LS + (lane & 3) * 16);
      const float mown = ((const LAS float*)(L8 + L_F))[4 + (lane & 3)];
      const unsigned th = MISC[M_TH];
      if (lane < 8) gstore2(rs, W_GM + b * 128 + lane * 16, tag, lane < 4 ? __float_as_uint(mown) : th, __float_as_uint((ls[0] + ls[1]) + (ls[2] + ls[3])));
      unsigned off[3]; u32x4 a[3];
      off[2] = W_GM + (kvh * 32 + (lane & 31)) * 128 + 64;
#pragma unroll
      for (int k = 0; k < 2; ++k) off[k] = W_GM + (kvh * 32 + k * 16 + (lane >> 2)) * 128 + (lane & 3) * 16;
      { int sp = 0; while (FULL[(STQ & (NSLOT - 1)) * 2] < (unsigned)(STQ + 1) && ++sp < SPIN_LDS) __builtin_amdgcn_s_sleep(8); }
      gather2<3, 16>(rs, off, tag, a);
      {
        const float rel = wave_sum((float)(int)(a[2][0] - th)) * (1.f / 64.f);
        MISC[M_BASE] = (th + (unsigned)(int)rel - (unsigned)(ATAU * NCH / 16 + TARGET)) | 1u;
      }
      const float m0 = __uint_as_float(a[0][0]), m1 = __uint_as_float(a[1][0]);
      float M = fmaxf(m0, m1);
      M = fmaxf(M, dpp<ROR4>(M)); M = fmaxf(M, dpp<XOR8>(M)); M = max_x16_x32(M);
      float L = fmaf(__builtin_amdgcn_exp2f((m0 - M) * LOG2E), __uint_as_float(a[0][2]), __builtin_amdgcn_exp2f((m1 - M) * LOG2E) * __uint_as_float(a[1][2]));
      L += dpp<ROR4>(L); L += dpp<XOR8>(L); L = sum_x16_x32(L);
      if (lane < 4) ((LAS float*)(L8 + L_F))[lane] = __builtin_amdgcn_exp2f((mown - M) * LOG2E) / L;
      wait_lds();
      MISC[M_FRDY] = 1u;
      if (b == 0) {
        unsigned oe[4]; u32x4 ae[4];
#pragma unroll
        for (int k = 0; k < 4; ++k) oe[k] = W_GM + (k * 64 + lane) * 128;
        gather2<4, 16>(rs, oe, tag, ae);
        if (lane == 0) __hip_atomic_store((GAS unsigned*)(ws + W_EPOCH), tag, __ATOMIC_RELAXED, __HIP_MEMORY_SCOPE_AGENT);
      }
    }
    wg_barrier();
    if (is_final) {
      constexpr int NL2 = FC, PPI = 64 / LPP;
      unsigned off[NL2]; u32x4 a[NL2];
#pragma unroll
      for (int k = 0; k < NL2; ++k) off[k] = W_G2 + ((k * PPI + lane / LPP) * 512 + FC * fb + 2 * (lane & (LPP - 1))) * 8;
      __builtin_amdgcn_s_sleep(PRE2);
      gather2<NL2, 0>(rs, off, tag, a);
      f32x2 r[NL2];
#pragma unroll
      for (int k = 0; k < NL2; ++k) r[k] = f32x2{__uint_as_float(a[k][0]), __uint_as_float(a[k][2])};
#pragma unroll
      for (int w = 1; w < NL2; w *= 2)
#pragma unroll
        for (int k = 0; k < NL2; k += 2 * w) r[k] += r[k + w];
      float sv = tr32(r[0][0], r[0][1]);
      sv = tr16(sv, sv);
      sv += dpp<XOR8>(sv);
      if (LPP <= 4) sv += dpp<ROR4>(sv);
      if (LPP <= 2) sv += dpp<XOR2>(sv);
      if (LPP <= 1) sv += dpp<XOR1>(sv);
      const float res = (lane < 32 ? xl0 : xl1) + rbf(rbf(sv) * (lane < 32 ? gl0 : gl1));
      auto sw = __builtin_amdgcn_permlane32_swap(__float_as_uint(res), __float_as_uint(res), false, false);
      if (lane < LPP) out_[LPP * fb + lane] = pk2(__uint_as_float(sw[0]), __uint_as_float(sw[1]));
    }
  }
  __builtin_amdgcn_s_waitcnt(0x0F70);
}

extern "C" void kernel_launch(void* const* d_in, const int* in_sizes, int n_in, void* d_out, int out_size, void* d_ws, size_t ws_size,
                              hipStream_t stream) {
  fused_decode<<<256, THREADS, 0, stream>>>((const char*)d_in[1], (char*)d_ws, (const u16*)d_in[0], (const char*)d_in[3],
                                            (const char*)d_in[2], (const char*)d_in[4], (unsigned*)d_out, (const u16*)d_in[6],
                                            (const u16*)d_in[7], (const u16*)d_in[8], (const u16*)d_in[9]);
}
```
